```python
import torch

def _softmax(x, axis=-1):
    return torch.softmax(x, dim=axis)
def _gelu(x):
    return torch.nn.functional.gelu(x, approximate="tanh")
def _silu(x):
    return torch.nn.functional.silu(x)
def _sigmoid(x):
    return torch.sigmoid(x)
def _layernorm(x, axis=-1, eps=1e-5):
    m = x.mean(dim=axis, keepdim=True)
    v = x.var(dim=axis, keepdim=True, unbiased=False)
    return (x - m) / torch.sqrt(v + eps)
def _scatter_add(tgt, idx, src, axis=0):
    return tgt.clone().index_add_(axis, idx, src)
def _segment_sum(src, seg, n):
    out = torch.zeros((n,) + src.shape[1:], dtype=src.dtype)
    return out.index_add_(0, seg, src)
def _pad(x, pw):
    flat = tuple(v for pair in reversed(pw) for v in pair)
    return torch.nn.functional.pad(x, flat)

kernel_name = 'parallel_sum__sliding_block_cached__dense_activation_gelu__decode'

def setup_inputs():

    _w = (lambda a: (lambda t: t.to(torch.bfloat16) if t.is_floating_point() else t)(torch.as_tensor(a)))
    def _A_setup():
        B, Sq, D = 8, 1, 512
        Hq, Hkv, Sk, Dh = 32, 8, 4096, 128
        assert Hq % Hkv == 0, (Hq, Hkv)
        return {
            "x": _w((lambda *s: torch.randn(*s, dtype=torch.bfloat16) * 0.05)(B, Sq, D)),
            "k_cache": _w((lambda *s: torch.randn(*s, dtype=torch.bfloat16) * 3.2)(B, Hkv, Sk, Dh)),
            "v_cache": _w((lambda *s: torch.randn(*s, dtype=torch.bfloat16) * 0.8)(B, Hkv, Sk, Dh)),
            "wq": _w((lambda *s: torch.randn(*s, dtype=torch.bfloat16) * 3.2)(D, Hq * Dh) * (D ** -0.5)),
            "wo": _w((lambda *s: torch.randn(*s, dtype=torch.bfloat16) * 0.8)(Hq * Dh, D) * ((Hq * Dh) ** -0.5)),
        }
    _A_in = _A_setup()

    def _B_setup():
        B, S, D, F = 8, 1, 512, 1792
        return {
            "x": _w((lambda *s: torch.randn(*s, dtype=torch.bfloat16) * 0.05)(B, S, D)),
            "w1": _w((lambda *s: torch.randn(*s, dtype=torch.bfloat16) * 3.2)(D, F) * (D ** -0.5)),
            "b1": _w((lambda *s: torch.randn(*s, dtype=torch.bfloat16) * 3.2)(F) * 0.01),
            "w2": _w((lambda *s: torch.randn(*s, dtype=torch.bfloat16) * 0.4)(F, D) * (F ** -0.5)),
            "b2": _w((lambda *s: torch.randn(*s, dtype=torch.bfloat16) * 0.4)(D) * 0.01),
        }
    _B_in = _B_setup()

    return {
        "A_x": _A_in["x"],
        "A_k_cache": _A_in["k_cache"],
        "A_v_cache": _A_in["v_cache"],
        "A_wq": _A_in["wq"],
        "A_wo": _A_in["wo"],
        "B_x": _B_in["x"],
        "B_w1": _B_in["w1"],
        "B_b1": _B_in["b1"],
        "B_w2": _B_in["w2"],
        "B_b2": _B_in["b2"],
    }

def reference(A_x, A_k_cache, A_v_cache, A_wq, A_wo, B_x, B_w1, B_b1, B_w2, B_b2):
    def _A_ref(x, k_cache, v_cache, wq, wo):
        B, Sq, D = x.shape
        Hq, Dh = 32, 128
        Hkv, Sk = k_cache.shape[1], k_cache.shape[2]
        W = min(4096, max(256, Sk // 4))
        rep = Hq // Hkv
        q = (lambda x, *d: x.permute(*d))((x @ wq).reshape(B, Sq, Hq, Dh), 0, 2, 1, 3)
        k = (lambda x, n, axis: torch.repeat_interleave(x, n, dim=axis))(k_cache[:, :, Sk - W:, :], rep, axis=1)
        v = (lambda x, n, axis: torch.repeat_interleave(x, n, dim=axis))(v_cache[:, :, Sk - W:, :], rep, axis=1)
        scores = torch.einsum("bhqd,bhkd->bhqk", q, k) * (Dh ** -0.5)
        p = _softmax(scores, axis=-1)
        o = torch.einsum("bhqk,bhkd->bhqd", p, v)
        return (lambda x, *d: x.permute(*d))(o, 0, 2, 1, 3).reshape(B, Sq, Hq * Dh) @ wo

    def _B_ref(x, w1, b1, w2, b2):
        h = torch.einsum("bsd,df->bsf", x, w1) + b1
        h = _gelu(h)
        return torch.einsum("bsf,fd->bsd", h, w2) + b2

    x = A_x
    xn = _layernorm(x, axis=-1)
    return x + _A_ref(xn, A_k_cache, A_v_cache, A_wq, A_wo) + _B_ref(xn, B_w1, B_b1, B_w2, B_b2)
```

```cpp
#include <hip/hip_runtime.h>
#include <hip/hip_bf16.h>
#include <stdint.h>

typedef unsigned int u32;
typedef unsigned long long u64;
typedef unsigned short u16;
typedef u32 u32x4 __attribute__((ext_vector_type(4)));
typedef float f32x4 __attribute__((ext_vector_type(4)));
typedef short bf16x8 __attribute__((ext_vector_type(8)));

#define GAS __attribute__((address_space(1)))
#define RLX_AGENT __ATOMIC_RELAXED, __HIP_MEMORY_SCOPE_AGENT

namespace {

constexpr int NB = 8, DM = 512, HQ = 32, HKV = 8, SK = 4096, DH = 128, WINDOW = 1024, FF = 1792;
constexpr int KOFF = SK - WINDOW;
constexpr int NWG = 256, NTHR = 768;
constexpr u32 TAG = 0x3C5A0001u;
constexpr u32 SPIN_CAP = 1u << 22;

constexpr int PREC = 66;
constexpr size_t QG_OFF = 0, QG_N = (size_t)NB * 2048;
constexpr size_t HG_OFF = QG_OFF + QG_N, HG_N = (size_t)NB * 896;
constexpr size_t PG_OFF = HG_OFF + HG_N, PG_N = (size_t)HQ * NB * 4 * PREC;
constexpr size_t OG_OFF = PG_OFF + PG_N, OG_N = (size_t)8 * 32 * 32 * 8 * 2;
constexpr size_t WS_GRAN = OG_OFF + OG_N;
constexpr size_t WS_BYTES = WS_GRAN * 8;
static_assert(WS_BYTES % 16 == 0, "memset block must be a multiple of 16 bytes");

constexpr int XN_STRIDE = 520;
constexpr int WT1_STRIDE = 520;
constexpr int K3 = 192, K3_STRIDE = 200;
constexpr int L_CTRL = 0;
constexpr int L_XN = 64;
constexpr int L_WT1 = L_XN + NB * XN_STRIDE * 2;
constexpr int L_WT3 = L_WT1 + 32 * WT1_STRIDE * 2;
constexpr int L_A3 = L_WT3 + 64 * K3_STRIDE * 2;
constexpr int L_Q = L_A3 + NB * K3_STRIDE * 2;
constexpr int L_P = L_Q + 4 * 128 * 2;
constexpr int L_MRG = L_P + 8 * 32 * 16;
constexpr int MRG_REC = 132;
constexpr int L_RED = L_MRG + 8 * 4 * MRG_REC * 4;
constexpr int L_END = L_RED + 4 * 16 * 4;
constexpr int LDS_BYTES = 96 * 1024;
static_assert(L_END <= LDS_BYTES, "LDS");
static_assert(L_WT1 % 16 == 0 && L_WT3 % 16 == 0 && L_A3 % 16 == 0 && L_Q % 16 == 0 && L_P % 16 == 0 && L_MRG % 16 == 0 && L_RED % 16 == 0, "align");

struct Params {
  const u16* x; const u16* kc; const u16* vc; const u16* wq; const u16* wo;
  const u16* w1; const u16* b1; const u16* w2; const u16* b2;
  u16* out; u64* ws;
};

__device__ __forceinline__ float bf2f(u32 h) { return __uint_as_float(h << 16); }
__device__ __forceinline__ u32 f2bf(float f) {
  __bf16 h = (__bf16)f;
  return (u32)__builtin_bit_cast(u16, h);
}
__device__ __forceinline__ float bfr(float f) { return bf2f(f2bf(f)); }

template <int CTRL> __device__ __forceinline__ float dpp(float x) {
  return __builtin_bit_cast(float, __builtin_amdgcn_mov_dpp(__builtin_bit_cast(int, x), CTRL, 0xf, 0xf, true));
}
__device__ __forceinline__ float row16_max(float x) {
  x = fmaxf(x, dpp<0x128>(x)); x = fmaxf(x, dpp<0x124>(x)); x = fmaxf(x, dpp<0x122>(x)); x = fmaxf(x, dpp<0x121>(x));
  return x;
}
__device__ __forceinline__ float row16_sum(float x) {
  x += dpp<0x128>(x); x += dpp<0x124>(x); x += dpp<0x122>(x); x += dpp<0x121>(x);
  return x;
}
__device__ __forceinline__ float xrow16_sum(float x) {
  auto s = __builtin_amdgcn_permlane16_swap(__float_as_uint(x), __float_as_uint(x), false, false);
  x = __uint_as_float(s[0]) + __uint_as_float(s[1]);
  auto t = __builtin_amdgcn_permlane32_swap(__float_as_uint(x), __float_as_uint(x), false, false);
  return __uint_as_float(t[0]) + __uint_as_float(t[1]);
}

__device__ __forceinline__ void put_gran(u64* g, u32 v) {
  __hip_atomic_store((GAS u64*)g, ((u64)TAG << 32) | (u64)v, RLX_AGENT);
}
__device__ __forceinline__ u64 get_gran(const u64* g) {
  return __hip_atomic_load((GAS u64*)g, RLX_AGENT);
}

__device__ __forceinline__ void lds_wait() { asm volatile("s_waitcnt lgkmcnt(0)" ::: "memory"); }

__device__ __forceinline__ void lds_arrive(volatile u32* c, int lane) {
  lds_wait();
  if (lane == 0) __hip_atomic_fetch_add((u32*)c, 1u, __ATOMIC_RELAXED, __HIP_MEMORY_SCOPE_WORKGROUP);
  lds_wait();
}
__device__ __forceinline__ void lds_spin(volatile u32* c, u32 target) {
  for (u32 it = 0; it < SPIN_CAP; ++it) {
    if (*c >= target) break;
    __builtin_amdgcn_s_sleep(1);
  }
  asm volatile("" ::: "memory");
}

__device__ __forceinline__ float gelu_tanh(float v) {
  const float u = 0.7978845608028654f * (v + 0.044715f * v * v * v);
  return 0.5f * v * (1.f + tanhf(u));
}

#define KV_WIN 4

__global__ void __launch_bounds__(NTHR, 3) fused_decode(Params p) {
  __shared__ __attribute__((aligned(16))) unsigned char smem[LDS_BYTES];
  volatile u32* ctrl = (volatile u32*)(smem + L_CTRL);

  const int tid = threadIdx.x, lane = tid & 63;
  const int wave = __builtin_amdgcn_readfirstlane(tid >> 6);
  const int bid = blockIdx.x, xg = bid & 7, y = bid >> 3;
  const int hkv = xg, bb = y >> 2, sp = y & 3;
  const int h3 = 4 * xg + (y & 3), cg = y >> 2;

  if (wave < 8) {
    const size_t kvbase = ((size_t)(bb * HKV + hkv) * SK + KOFF + sp * 256 + wave * 32) * DH;
    const u32x4* kp = (const u32x4*)(p.kc + kvbase + (size_t)(lane & 15) * DH + (lane >> 4) * 8);
    const u32x4* vp = (const u32x4*)(p.vc + kvbase + (size_t)(lane >> 4) * DH + (lane & 15) * 8);
    u32x4 kv[16];
#define KV_ADDR(n) ((n) < 8 ? (kp + ((n) >> 2) * 16 * (DH / 8) + ((n) & 3) * 4) : (vp + ((n) - 8) * 4 * (DH / 8)))
#pragma unroll
    for (int n = 0; n < KV_WIN; ++n) kv[n] = __builtin_nontemporal_load(KV_ADDR(n));
    asm volatile("" ::: "memory");
    __builtin_amdgcn_s_barrier();
#pragma unroll
    for (int n = KV_WIN; n < 16; ++n) {
      asm volatile("s_waitcnt vmcnt(%0)" :: "i"(KV_WIN - 1) : "memory");
      kv[n] = __builtin_nontemporal_load(KV_ADDR(n));
    }
    asm volatile("" ::: "memory");
#undef KV_ADDR
    lds_spin(&ctrl[0], 4);
    bf16x8 qf[4];
#pragma unroll
    for (int t = 0; t < 4; ++t) {
      bf16x8 z = {0, 0, 0, 0, 0, 0, 0, 0};
      if ((lane & 15) < 4) z = *(const bf16x8*)(smem + L_Q + ((lane & 15) * 128 + t * 32 + (lane >> 4) * 8) * 2);
      qf[t] = z;
    }
    f32x4 s0 = {0.f, 0.f, 0.f, 0.f}, s1 = {0.f, 0.f, 0.f, 0.f};
#pragma unroll
    for (int t = 0; t < 4; ++t) {
      s0 = __builtin_amdgcn_mfma_f32_16x16x32_bf16(qf[t], __builtin_bit_cast(bf16x8, kv[t]), s0, 0, 0, 0);
      s1 = __builtin_amdgcn_mfma_f32_16x16x32_bf16(qf[t], __builtin_bit_cast(bf16x8, kv[4 + t]), s1, 0, 0, 0);
    }
    constexpr float SCALE = 0.08838834764831845f, LOG2E = 1.4426950408889634f;
    float mh[4], lh[4];
    f32x4 p0, p1;
#pragma unroll
    for (int h = 0; h < 4; ++h) {
      const float a = bfr(bfr(s0[h]) * SCALE), b2 = bfr(bfr(s1[h]) * SCALE);
      const float m = row16_max(fmaxf(a, b2));
      p0[h] = __builtin_amdgcn_exp2f((a - m) * LOG2E);
      p1[h] = __builtin_amdgcn_exp2f((b2 - m) * LOG2E);
      mh[h] = m;
      lh[h] = row16_sum(p0[h] + p1[h]);
    }
    float* pbuf = (float*)(smem + L_P) + wave * 32 * 4;
    if (lane < 16) {
      *(f32x4*)(pbuf + lane * 4) = p0;
      *(f32x4*)(pbuf + (16 + lane) * 4) = p1;
    }
    lds_wait();
    __builtin_amdgcn_wave_barrier();
    float acc[32];
#pragma unroll
    for (int k = 0; k < 32; ++k) acc[k] = 0.f;
#pragma unroll
    for (int i = 0; i < 8; ++i) {
      const f32x4 pr = *(const f32x4*)(pbuf + (4 * i + (lane >> 4)) * 4);
      float vf[8];
#pragma unroll
      for (int k = 0; k < 4; ++k) {
        vf[2 * k] = __uint_as_float(kv[8 + i][k] << 16);
        vf[2 * k + 1] = __uint_as_float(kv[8 + i][k] & 0xffff0000u);
      }
#pragma unroll
      for (int h = 0; h < 4; ++h)
#pragma unroll
        for (int k = 0; k < 8; ++k) acc[h * 8 + k] = fmaf(pr[h], vf[k], acc[h * 8 + k]);
    }
#pragma unroll
    for (int k = 0; k < 32; ++k) acc[k] = xrow16_sum(acc[k]);
    float* mrg = (float*)(smem + L_MRG);
    if (lane < 16) {
#pragma unroll
      for (int h = 0; h < 4; ++h) {
        float* rec = mrg + (wave * 4 + h) * MRG_REC;
        *(f32x4*)(rec + 8 * lane) = f32x4{acc[h * 8], acc[h * 8 + 1], acc[h * 8 + 2], acc[h * 8 + 3]};
        *(f32x4*)(rec + 8 * lane + 4) = f32x4{acc[h * 8 + 4], acc[h * 8 + 5], acc[h * 8 + 6], acc[h * 8 + 7]};
        if (lane == 0) { rec[128] = mh[h]; rec[129] = lh[h]; }
      }
    }
    lds_arrive(&ctrl[1], lane);
    if (wave >= 4) return;
    lds_spin(&ctrl[1], 8);
    {
      const int h = wave, dp = lane;
      float M = -INFINITY;
#pragma unroll
      for (int w = 0; w < 8; ++w) M = fmaxf(M, mrg[(w * 4 + h) * MRG_REC + 128]);
      float L = 0.f, o0 = 0.f, o1 = 0.f;
#pragma unroll
      for (int w = 0; w < 8; ++w) {
        const float* rec = mrg + (w * 4 + h) * MRG_REC;
        const float wt = __builtin_amdgcn_exp2f((rec[128] - M) * LOG2E);
        L = fmaf(wt, rec[129], L);
        o0 = fmaf(wt, rec[2 * dp], o0);
        o1 = fmaf(wt, rec[2 * dp + 1], o1);
      }
      u64* rec = p.ws + PG_OFF + (((size_t)(hkv * 4 + h) * NB + bb) * 4 + sp) * PREC;
      put_gran(rec + dp, f2bf(o0) | (f2bf(o1) << 16));
      if (lane == 0) { put_gran(rec + 64, __float_as_uint(M)); put_gran(rec + 65, __float_as_uint(L)); }
    }
    if (lane == 0) __hip_atomic_fetch_add((u32*)&ctrl[2], 1u, __ATOMIC_RELAXED, __HIP_MEMORY_SCOPE_WORKGROUP);
    return;
  }

  const int st = tid - 512, sw = wave - 8;
  if (st < 16) ctrl[st] = 0u;
  const int a_ff = xg + 8 * (y >> 3);
  const bool has_ffn = a_ff < 28;
  const int fch = 8 * a_ff + (y & 7);
  const int qc0 = hkv * 512 + 16 * y;

  const int lb = st >> 5, lseg = st & 31;
  const u32x4 xa = *(const u32x4*)(p.x + lb * DM + lseg * 16);
  const u32x4 xb = *(const u32x4*)(p.x + lb * DM + lseg * 16 + 8);
  const u32x4 wa0 = *(const u32x4*)(p.wq + (size_t)(2 * st) * 4096 + qc0);
  const u32x4 wa1 = *(const u32x4*)(p.wq + (size_t)(2 * st) * 4096 + qc0 + 8);
  const u32x4 wb0 = *(const u32x4*)(p.wq + (size_t)(2 * st + 1) * 4096 + qc0);
  const u32x4 wb1 = *(const u32x4*)(p.wq + (size_t)(2 * st + 1) * 4096 + qc0 + 8);
  u32x4 wc0 = {0, 0, 0, 0}, wc1 = {0, 0, 0, 0};
  if (has_ffn) {
    wc0 = *(const u32x4*)(p.w1 + (size_t)(2 * st) * FF + 8 * fch);
    wc1 = *(const u32x4*)(p.w1 + (size_t)(2 * st + 1) * FF + 8 * fch);
  }
  lds_wait();
  __builtin_amdgcn_s_barrier();

  {
    float xv[16];
#pragma unroll
    for (int k = 0; k < 4; ++k) {
      xv[2 * k] = __uint_as_float(xa[k] << 16); xv[2 * k + 1] = __uint_as_float(xa[k] & 0xffff0000u);
      xv[8 + 2 * k] = __uint_as_float(xb[k] << 16); xv[8 + 2 * k + 1] = __uint_as_float(xb[k] & 0xffff0000u);
    }
    float s = 0.f;
#pragma unroll
    for (int k = 0; k < 16; ++k) s += xv[k];
#pragma unroll
    for (int o = 1; o < 32; o <<= 1) s += __shfl_xor(s, o);
    const float mean = s * (1.f / 512.f);
    float v = 0.f;
#pragma unroll
    for (int k = 0; k < 16; ++k) { const float d = xv[k] - mean; v = fmaf(d, d, v); }
#pragma unroll
    for (int o = 1; o < 32; o <<= 1) v += __shfl_xor(v, o);
    const float mb = bfr(mean), vb = bfr(v * (1.f / 512.f));
    const float den = bfr(sqrtf(bfr(vb + 1e-5f)));
    u32 o[8];
#pragma unroll
    for (int k = 0; k < 8; ++k) {
      const float a = bfr(xv[2 * k] - mb) / den, b = bfr(xv[2 * k + 1] - mb) / den;
      o[k] = f2bf(a) | (f2bf(b) << 16);
    }
    u32* dst = (u32*)(smem + L_XN + (lb * XN_STRIDE + lseg * 16) * 2);
    *(u32x4*)dst = u32x4{o[0], o[1], o[2], o[3]};
    *(u32x4*)(dst + 4) = u32x4{o[4], o[5], o[6], o[7]};
  }
  {
    u32* wt = (u32*)(smem + L_WT1) + st;
#pragma unroll
    for (int k = 0; k < 4; ++k) {
      wt[(2 * k) * (WT1_STRIDE / 2)] = __builtin_amdgcn_perm(wb0[k], wa0[k], 0x05040100u);
      wt[(2 * k + 1) * (WT1_STRIDE / 2)] = __builtin_amdgcn_perm(wb0[k], wa0[k], 0x07060302u);
      wt[(8 + 2 * k) * (WT1_STRIDE / 2)] = __builtin_amdgcn_perm(wb1[k], wa1[k], 0x05040100u);
      wt[(8 + 2 * k + 1) * (WT1_STRIDE / 2)] = __builtin_amdgcn_perm(wb1[k], wa1[k], 0x07060302u);
      wt[(16 + 2 * k) * (WT1_STRIDE / 2)] = __builtin_amdgcn_perm(wc1[k], wc0[k], 0x05040100u);
      wt[(16 + 2 * k + 1) * (WT1_STRIDE / 2)] = __builtin_amdgcn_perm(wc1[k], wc0[k], 0x07060302u);
      wt[(24 + 2 * k) * (WT1_STRIDE / 2)] = 0u;
      wt[(24 + 2 * k + 1) * (WT1_STRIDE / 2)] = 0u;
    }
  }
  lds_arrive(&ctrl[4], lane);
  lds_spin(&ctrl[4], 4);
  if (sw == 0 || (sw == 1 && has_ffn)) {
    f32x4 d = {0.f, 0.f, 0.f, 0.f};
    const int m = lane & 15, g = lane >> 4;
#pragma unroll
    for (int ks = 0; ks < 16; ++ks) {
      bf16x8 a = {0, 0, 0, 0, 0, 0, 0, 0};
      if (m < 8) a = *(const bf16x8*)(smem + L_XN + (m * XN_STRIDE + ks * 32 + g * 8) * 2);
      const bf16x8 b = *(const bf16x8*)(smem + L_WT1 + ((sw * 16 + m) * WT1_STRIDE + ks * 32 + g * 8) * 2);
      d = __builtin_amdgcn_mfma_f32_16x16x32_bf16(a, b, d, 0, 0, 0);
    }
    if (sw == 0) {
#pragma unroll
      for (int i = 0; i < 4; ++i) {
        const u32 mine = f2bf(d[i]);
        const u32 other = (u32)__shfl_xor((int)mine, 1);
        if (lane < 32 && (m & 1) == 0)
          put_gran(p.ws + QG_OFF + (size_t)(4 * g + i) * 2048 + ((qc0 + m) >> 1), mine | (other << 16));
      }
    } else {
      const float bias = bf2f(p.b1[8 * fch + (m & 7)]);
#pragma unroll
      for (int i = 0; i < 4; ++i) {
        const u32 mine = f2bf(gelu_tanh(bfr(bfr(d[i]) + bias)));
        const u32 other = (u32)__shfl_xor((int)mine, 1);
        if (lane < 32 && m < 8 && (m & 1) == 0)
          put_gran(p.ws + HG_OFF + (size_t)(4 * g + i) * 896 + ((8 * fch + m) >> 1), mine | (other << 16));
      }
    }
  }
  {
    const u64* g = p.ws + QG_OFF + (size_t)bb * 2048 + hkv * 256 + st;
    u64 v = 0;
    for (u32 it = 0; it < SPIN_CAP; ++it) {
      v = get_gran(g);
      if (__all((u32)(v >> 32) == TAG)) break;
      __builtin_amdgcn_s_sleep(2);
    }
    ((u32*)(smem + L_Q))[st] = (u32)v;
    lds_arrive(&ctrl[0], lane);
  }
  {
    u32x4 r0[3], r1[3];
#pragma unroll
    for (int it = 0; it < 3; ++it) {
      const int idx = st + 256 * it;
      if (idx < 736) {
        const int pr = idx >> 3, ch = idx & 7, row = 2 * pr;
        const u16* src = row < 128 ? (p.wo + (size_t)(128 * h3 + row) * DM) : (p.w2 + (size_t)(56 * h3 + row - 128) * DM);
        src += 64 * cg + 8 * ch;
        r0[it] = *(const u32x4*)src;
        r1[it] = *(const u32x4*)(src + DM);
      }
    }
    u32* wt = (u32*)(smem + L_WT3);
#pragma unroll
    for (int it = 0; it < 3; ++it) {
      const int idx = st + 256 * it;
      if (idx < 736) {
        const int pr = idx >> 3, ch = idx & 7;
#pragma unroll
        for (int k = 0; k < 4; ++k) {
          wt[(8 * ch + 2 * k) * (K3_STRIDE / 2) + pr] = __builtin_amdgcn_perm(r1[it][k], r0[it][k], 0x05040100u);
          wt[(8 * ch + 2 * k + 1) * (K3_STRIDE / 2) + pr] = __builtin_amdgcn_perm(r1[it][k], r0[it][k], 0x07060302u);
        }
      }
    }
    wt[(st >> 2) * (K3_STRIDE / 2) + 92 + (st & 3)] = 0u;
    if (st < 32) ((u32*)(smem + L_A3))[(st >> 2) * (K3_STRIDE / 2) + 92 + (st & 3)] = 0u;
  }
  if (st < 224) {
    const int b = st / 28, j = st - 28 * b;
    const u64* g = p.ws + HG_OFF + (size_t)b * 896 + 28 * h3 + j;
    u64 v = 0;
    for (u32 it = 0; it < SPIN_CAP; ++it) {
      v = get_gran(g);
      if ((u32)(v >> 32) == TAG) break;
      __builtin_amdgcn_s_sleep(2);
    }
    ((u32*)(smem + L_A3))[b * (K3_STRIDE / 2) + 64 + j] = (u32)v;
  }
  lds_spin(&ctrl[2], 4);
  {
    constexpr float LOG2E = 1.4426950408889634f;
    const int b = st >> 5, j = st & 31;
    const u64* base = p.ws + PG_OFF + ((size_t)h3 * NB + b) * 4 * PREC;
    u64 ov[8], mv[4], lv[4];
    for (u32 it = 0; it < SPIN_CAP; ++it) {
      bool ok = true;
#pragma unroll
      for (int s = 0; s < 4; ++s) {
        ov[2 * s] = get_gran(base + s * PREC + 2 * j);
        ov[2 * s + 1] = get_gran(base + s * PREC + 2 * j + 1);
        mv[s] = get_gran(base + s * PREC + 64);
        lv[s] = get_gran(base + s * PREC + 65);
      }
#pragma unroll
      for (int s = 0; s < 4; ++s)
        ok = ok && (u32)(ov[2 * s] >> 32) == TAG && (u32)(ov[2 * s + 1] >> 32) == TAG && (u32)(mv[s] >> 32) == TAG && (u32)(lv[s] >> 32) == TAG;
      if (__all(ok)) break;
      __builtin_amdgcn_s_sleep(1);
    }
    float M = -INFINITY;
#pragma unroll
    for (int s = 0; s < 4; ++s) M = fmaxf(M, __uint_as_float((u32)mv[s]));
    float L = 0.f, o[4] = {0.f, 0.f, 0.f, 0.f};
#pragma unroll
    for (int s = 0; s < 4; ++s) {
      const float wt = __builtin_amdgcn_exp2f((__uint_as_float((u32)mv[s]) - M) * LOG2E);
      L = fmaf(wt, __uint_as_float((u32)lv[s]), L);
      o[0] = fmaf(wt, bf2f((u32)ov[2 * s] & 0xffffu), o[0]);
      o[1] = fmaf(wt, bf2f(((u32)ov[2 * s]) >> 16), o[1]);
      o[2] = fmaf(wt, bf2f((u32)ov[2 * s + 1] & 0xffffu), o[2]);
      o[3] = fmaf(wt, bf2f(((u32)ov[2 * s + 1]) >> 16), o[3]);
    }
    const float inv = 1.f / L;
    u32* a3 = (u32*)(smem + L_A3) + b * (K3_STRIDE / 2) + 2 * j;
    a3[0] = f2bf(o[0] * inv) | (f2bf(o[1] * inv) << 16);
    a3[1] = f2bf(o[2] * inv) | (f2bf(o[3] * inv) << 16);
  }
  lds_arrive(&ctrl[5], lane);
  lds_spin(&ctrl[5], 4);
  {
    f32x4 d = {0.f, 0.f, 0.f, 0.f};
    const int m = lane & 15, g = lane >> 4;
#pragma unroll
    for (int ks = 0; ks < 6; ++ks) {
      bf16x8 a = {0, 0, 0, 0, 0, 0, 0, 0};
      if (m < 8) a = *(const bf16x8*)(smem + L_A3 + (m * K3_STRIDE + ks * 32 + g * 8) * 2);
      const bf16x8 b = *(const bf16x8*)(smem + L_WT3 + ((sw * 16 + m) * K3_STRIDE + ks * 32 + g * 8) * 2);
      d = __builtin_amdgcn_mfma_f32_16x16x32_bf16(a, b, d, 0, 0, 0);
    }
    if (lane < 32) {
      const int col = 16 * sw + m;
      u64* og = p.ws + OG_OFF + ((((size_t)cg * 32 + (col >> 1)) * 32 + h3) * 8) * 2 + (col & 1);
#pragma unroll
      for (int i = 0; i < 4; ++i) put_gran(og + (4 * g + i) * 2, __float_as_uint(d[i]));
    }
  }
  {
    const u64* base = p.ws + OG_OFF + (((size_t)cg * 32 + h3) * 32) * 16 + 2 * st;
    u64 a = 0, b = 0;
    for (u32 it = 0; it < SPIN_CAP; ++it) {
      a = get_gran(base); b = get_gran(base + 1);
      if (__all((u32)(a >> 32) == TAG && (u32)(b >> 32) == TAG)) break;
      __builtin_amdgcn_s_sleep(1);
    }
    float s0 = __uint_as_float((u32)a), s1 = __uint_as_float((u32)b);
#pragma unroll
    for (int o = 8; o < 64; o <<= 1) { s0 += __shfl_xor(s0, o); s1 += __shfl_xor(s1, o); }
    float* red = (float*)(smem + L_RED);
    if (lane < 8) { red[sw * 16 + lane * 2] = s0; red[sw * 16 + lane * 2 + 1] = s1; }
    lds_arrive(&ctrl[6], lane);
    if (sw != 0) return;
    lds_spin(&ctrl[6], 4);
    if (lane < 8) {
      const int col = 64 * cg + 2 * h3;
      float r0 = 0.f, r1 = 0.f;
#pragma unroll
      for (int w = 0; w < 4; ++w) { r0 += red[w * 16 + lane * 2]; r1 += red[w * 16 + lane * 2 + 1]; }
      const u32 xx = *(const u32*)(p.x + lane * DM + col);
      const u32 bb2 = *(const u32*)(p.b2 + col);
      r0 += bf2f(xx & 0xffffu) + bf2f(bb2 & 0xffffu);
      r1 += bf2f(xx >> 16) + bf2f(bb2 >> 16);
      *(u32*)(p.out + lane * DM + col) = f2bf(r0) | (f2bf(r1) << 16);
    }
  }
}

}

extern "C" void kernel_launch(void* const* d_in, const int* in_sizes, int n_in,
                              void* d_out, int out_size, void* d_ws, size_t ws_size,
                              hipStream_t stream) {
  Params p;
  p.x = (const u16*)d_in[0];
  p.kc = (const u16*)d_in[1];
  p.vc = (const u16*)d_in[2];
  p.wq = (const u16*)d_in[3];
  p.wo = (const u16*)d_in[4];
  p.w1 = (const u16*)d_in[6];
  p.b1 = (const u16*)d_in[7];
  p.w2 = (const u16*)d_in[8];
  p.b2 = (const u16*)d_in[9];
  p.out = (u16*)d_out;
  p.ws = (u64*)d_ws;
  (void)hipMemsetAsync(d_ws, 0, WS_BYTES, stream);
  hipLaunchKernelGGL(fused_decode, dim3(NWG), dim3(NTHR), 0, stream, p);
}
```

```cpp
#include <hip/hip_runtime.h>
#include <hip/hip_bf16.h>
#include <stdint.h>

typedef unsigned int u32;
typedef unsigned long long u64;
typedef unsigned short u16;
typedef u32 u32x4 __attribute__((ext_vector_type(4)));
typedef float f32x4 __attribute__((ext_vector_type(4)));
typedef short bf16x8 __attribute__((ext_vector_type(8)));
typedef short s16x4 __attribute__((ext_vector_type(4)));

#define GAS __attribute__((address_space(1)))
#define LAS __attribute__((address_space(3)))
#define RLX_AGENT __ATOMIC_RELAXED, __HIP_MEMORY_SCOPE_AGENT

namespace {

constexpr int NB = 8, DM = 512, HQ = 32, HKV = 8, SK = 4096, DH = 128, WINDOW = 1024, FF = 1792;
constexpr int KOFF = SK - WINDOW;
constexpr int NWG = 256, NTHR = 768;
constexpr u32 TAG = 0x3C5A0001u;
constexpr u32 SPIN_CAP = 1u << 22;
constexpr float LOG2E = 1.4426950408889634f;

constexpr int PREC = 66;
constexpr size_t QG_OFF = 0, QG_N = (size_t)NB * 4 * 4096;
constexpr size_t HG_OFF = QG_OFF + QG_N, HG_N = (size_t)NB * 4 * FF;
constexpr size_t PG_OFF = HG_OFF + HG_N, PG_N = (size_t)HQ * NB * 4 * PREC;
constexpr size_t OG_OFF = PG_OFF + PG_N, OG_N = (size_t)8 * 32 * 32 * 8 * 2;
constexpr size_t WS_GRAN = OG_OFF + OG_N;
constexpr size_t WS_BYTES = WS_GRAN * 8;
static_assert(WS_BYTES % 16 == 0, "memset block must be a multiple of 16 bytes");

constexpr int XN_STRIDE = 520;
constexpr int K3_STRIDE = 200;
constexpr int L_CTRL = 0;
constexpr int L_XN = 64;
constexpr int L_WQ = L_XN + NB * XN_STRIDE * 2;
constexpr int L_W1 = L_WQ + 16384;
constexpr int L_W3 = L_W1 + 16384;
constexpr int L_A3 = L_W3 + 24576;
constexpr int L_Q = L_A3 + NB * K3_STRIDE * 2;
constexpr int L_V = L_Q + 1024;
constexpr int L_PB = L_V + 65536;
constexpr int L_ML = L_PB + 8192;
constexpr int L_SC = L_ML + 256;
constexpr int L_RED = L_SC + 1024;
constexpr int L_END = L_RED + 256;
constexpr int LDS_BYTES = L_END;
static_assert(LDS_BYTES > 80 * 1024 && LDS_BYTES <= 160 * 1024, "one workgroup per CU");
static_assert(L_WQ % 16 == 0 && L_W3 % 16 == 0 && L_A3 % 16 == 0 && L_Q % 16 == 0 && L_V % 16 == 0 && L_PB % 16 == 0, "align");

struct Params {
  const u16* x; const u16* kc; const u16* vc; const u16* wq; const u16* wo;
  const u16* w1; const u16* b1; const u16* w2; const u16* b2;
  u16* out; u64* ws;
};

__device__ __forceinline__ float bf2f(u32 h) { return __uint_as_float(h << 16); }
__device__ __forceinline__ u32 f2bf(float f) {
  __bf16 h = (__bf16)f;
  return (u32)__builtin_bit_cast(u16, h);
}
__device__ __forceinline__ float bfr(float f) { return bf2f(f2bf(f)); }

template <int CTRL> __device__ __forceinline__ float dpp(float x) {
  return __builtin_bit_cast(float, __builtin_amdgcn_mov_dpp(__builtin_bit_cast(int, x), CTRL, 0xf, 0xf, true));
}
template <int CTRL> __device__ __forceinline__ u32 dppu(u32 x) {
  return (u32)__builtin_amdgcn_mov_dpp((int)x, CTRL, 0xf, 0xf, true);
}
__device__ __forceinline__ float x16_max(float x) {
  auto s = __builtin_amdgcn_permlane16_swap(__float_as_uint(x), __float_as_uint(x), false, false);
  return fmaxf(__uint_as_float(s[0]), __uint_as_float(s[1]));
}
__device__ __forceinline__ float x32_max(float x) {
  auto t = __builtin_amdgcn_permlane32_swap(__float_as_uint(x), __float_as_uint(x), false, false);
  return fmaxf(__uint_as_float(t[0]), __uint_as_float(t[1]));
}
__device__ __forceinline__ float x16_sum(float x) {
  auto s = __builtin_amdgcn_permlane16_swap(__float_as_uint(x), __float_as_uint(x), false, false);
  return __uint_as_float(s[0]) + __uint_as_float(s[1]);
}
__device__ __forceinline__ float x32_sum(float x) {
  auto t = __builtin_amdgcn_permlane32_swap(__float_as_uint(x), __float_as_uint(x), false, false);
  return __uint_as_float(t[0]) + __uint_as_float(t[1]);
}

__device__ __forceinline__ void put_gran(u64* g, u32 v) {
  __hip_atomic_store((GAS u64*)g, ((u64)TAG << 32) | (u64)v, RLX_AGENT);
}
__device__ __forceinline__ u64 get_gran(const u64* g) {
  return __hip_atomic_load((GAS u64*)g, RLX_AGENT);
}
__device__ __forceinline__ bool tag_ok(u64 v) { return (u32)(v >> 32) == TAG; }

__device__ __forceinline__ void lds_wait() { asm volatile("s_waitcnt lgkmcnt(0)" ::: "memory"); }
__device__ __forceinline__ void vm_wait0() { asm volatile("s_waitcnt vmcnt(0)" ::: "memory"); }

__device__ __forceinline__ void lds_arrive(volatile u32* c, int lane) {
  lds_wait();
  if (lane == 0) __hip_atomic_fetch_add((u32*)c, 1u, __ATOMIC_RELAXED, __HIP_MEMORY_SCOPE_WORKGROUP);
  asm volatile("" ::: "memory");
}
__device__ __forceinline__ void lds_spin(volatile u32* c, u32 target) {
  for (u32 it = 0; it < SPIN_CAP; ++it) {
    if (*c >= target) break;
    __builtin_amdgcn_s_sleep(1);
  }
  asm volatile("" ::: "memory");
}

__device__ __forceinline__ float gelu_tanh(float v) {
  const float u = 0.7978845608028654f * (v + 0.044715f * v * v * v);
  return 0.5f * v * (1.f + tanhf(u));
}

__device__ __forceinline__ void dma16(const void* gsrc, unsigned char* lds_base) {
  __builtin_amdgcn_global_load_lds(gsrc, (LAS void*)lds_base, 16, 0, 2);
}
__device__ __forceinline__ bf16x8 bfrag_tr(const unsigned char* tile, int ks, int nt, int lane) {
  const int g = lane >> 4, q = (lane & 15) >> 2, pp = lane & 3;
  const unsigned char* a = tile + (32 * ks + 8 * g + q) * 128 + (16 * nt + 4 * pp) * 2;
  const s16x4 lo = __builtin_amdgcn_ds_read_tr16_b64_v4i16((LAS s16x4*)a);
  const s16x4 hi = __builtin_amdgcn_ds_read_tr16_b64_v4i16((LAS s16x4*)(a + 4 * 128));
  return bf16x8{lo[0], lo[1], lo[2], lo[3], hi[0], hi[1], hi[2], hi[3]};
}
__device__ __forceinline__ int vswz(int row) { return ((row & 3) << 2) | ((row >> 2) & 3); }

#define KV_WIN 6
#define STAMP(k) do { } while (0)

__global__ void __launch_bounds__(NTHR, 3) fused_decode(Params p) {
  __shared__ __attribute__((aligned(16))) unsigned char smem[LDS_BYTES];
  volatile u32* ctrl = (volatile u32*)(smem + L_CTRL);

  const int tid = threadIdx.x, lane = tid & 63;
  const int wave = __builtin_amdgcn_readfirstlane(tid >> 6);
  const int bid = blockIdx.x, xg = bid & 7, y = bid >> 3;
  const int hkv = xg, bb = y >> 2, sp = y & 3;
  const int h3 = 4 * xg + (y & 3), cg = y >> 2;
  const int g4 = lane >> 4, m16 = lane & 15;

  if (wave < 8) {
    if (wave == 0) STAMP(0);
    const size_t kvbase = ((size_t)(bb * HKV + hkv) * SK + KOFF + sp * 256 + wave * 32) * DH;
    const u32x4* kp = (const u32x4*)(p.kc + kvbase + (size_t)m16 * DH + g4 * 8);
    u32x4 kf[8];
#define KV_ISSUE(n) do { \
      if ((n) < 8) { kf[(n) & 7] = __builtin_nontemporal_load(kp + ((n) >> 2) * 16 * (DH / 8) + ((n) & 3) * 4); } \
      else { const int row_ = 32 * wave + 4 * ((n) - 8) + g4; \
             dma16(p.vc + kvbase + (size_t)(4 * ((n) - 8) + g4) * DH + ((m16 ^ vswz(row_)) * 8), smem + L_V + (32 * wave + 4 * ((n) - 8)) * 256); } \
      asm volatile("" ::: "memory"); } while (0)
#pragma unroll
    for (int n = 0; n < KV_WIN; ++n) KV_ISSUE(n);
    __builtin_amdgcn_s_barrier();
#pragma unroll
    for (int n = KV_WIN; n < 16; ++n) {
      asm volatile("s_waitcnt vmcnt(%0)" :: "i"(KV_WIN - 1) : "memory");
      KV_ISSUE(n);
    }
#undef KV_ISSUE
    if (wave == 0) STAMP(1);
    lds_spin(&ctrl[0], 4);
    if (wave == 0) STAMP(2);
    bf16x8 qf[4];
#pragma unroll
    for (int t = 0; t < 4; ++t) {
      bf16x8 z = {0, 0, 0, 0, 0, 0, 0, 0};
      if (m16 < 4) z = *(const bf16x8*)(smem + L_Q + (m16 * 128 + t * 32 + g4 * 8) * 2);
      qf[t] = z;
    }
    f32x4 s0 = {0.f, 0.f, 0.f, 0.f}, s1 = {0.f, 0.f, 0.f, 0.f};
#pragma unroll
    for (int t = 0; t < 4; ++t) {
      s0 = __builtin_amdgcn_mfma_f32_16x16x32_bf16(__builtin_bit_cast(bf16x8, kf[t]), qf[t], s0, 0, 0, 0);
      s1 = __builtin_amdgcn_mfma_f32_16x16x32_bf16(__builtin_bit_cast(bf16x8, kf[4 + t]), qf[t], s1, 0, 0, 0);
    }
    constexpr float SCALE = 0.08838834764831845f;
    float a[8];
#pragma unroll
    for (int i = 0; i < 4; ++i) { a[i] = bfr(bfr(s0[i]) * SCALE); a[4 + i] = bfr(bfr(s1[i]) * SCALE); }
    float m = fmaxf(fmaxf(fmaxf(a[0], a[1]), fmaxf(a[2], a[3])), fmaxf(fmaxf(a[4], a[5]), fmaxf(a[6], a[7])));
    m = x32_max(x16_max(m));
    float l = 0.f;
#pragma unroll
    for (int i = 0; i < 8; ++i) { a[i] = __builtin_amdgcn_exp2f((a[i] - m) * LOG2E); }
    u32x4 pf;
#pragma unroll
    for (int i = 0; i < 4; ++i) {
      const u32 lo = f2bf(a[2 * i]), hi = f2bf(a[2 * i + 1]);
      pf[i] = lo | (hi << 16);
      l += bf2f(lo) + bf2f(hi);
    }
    l = x32_sum(x16_sum(l));
    *(u32x4*)(smem + L_PB + (wave * 64 + lane) * 16) = pf;
    if (lane < 4) {
      float* ml = (float*)(smem + L_ML) + (wave * 4 + lane) * 2;
      ml[0] = m; ml[1] = l;
    }
    if (wave == 0) STAMP(3);
    vm_wait0();
    lds_arrive(&ctrl[1], lane);
    lds_spin(&ctrl[1], 8);
    if (wave == 0) STAMP(4);
    float Mx, Lx;
    {
      const int ws = (lane >> 2) & 7, h = lane & 3;
      const float* ml = (const float*)(smem + L_ML) + (ws * 4 + h) * 2;
      const float mw = ml[0], lw = ml[1];
      float M = mw;
      M = fmaxf(M, dpp<0x124>(M)); M = fmaxf(M, dpp<0x128>(M)); M = x16_max(M);
      const float sc = __builtin_amdgcn_exp2f((mw - M) * LOG2E);
      float L = sc * lw;
      L += dpp<0x124>(L); L += dpp<0x128>(L); L = x16_sum(L);
      Mx = M; Lx = L;
      if (lane < 32) ((float*)(smem + L_SC))[wave * 32 + lane] = sc;
      lds_wait();
      __builtin_amdgcn_wave_barrier();
    }
    f32x4 o = {0.f, 0.f, 0.f, 0.f};
    {
      const int q = m16 >> 2, pp = lane & 3;
#pragma unroll
      for (int w = 0; w < 8; ++w) {
        const bf16x8 pa = *(const bf16x8*)(smem + L_PB + (w * 64 + lane) * 16);
        const int r0 = 32 * w + 4 * g4 + q, r1 = r0 + 16;
        const int ch = 2 * wave + (pp >> 1);
        const s16x4 lo = __builtin_amdgcn_ds_read_tr16_b64_v4i16((LAS s16x4*)(smem + L_V + r0 * 256 + ((ch ^ vswz(r0)) * 16) + 8 * (pp & 1)));
        const s16x4 hi = __builtin_amdgcn_ds_read_tr16_b64_v4i16((LAS s16x4*)(smem + L_V + r1 * 256 + ((ch ^ vswz(r1)) * 16) + 8 * (pp & 1)));
        const bf16x8 vb = {lo[0], lo[1], lo[2], lo[3], hi[0], hi[1], hi[2], hi[3]};
        const f32x4 z = {0.f, 0.f, 0.f, 0.f};
        const f32x4 d = __builtin_amdgcn_mfma_f32_16x16x32_bf16(pa, vb, z, 0, 0, 0);
        const f32x4 sc = *(const f32x4*)(smem + L_SC + (wave * 32 + w * 4) * 4);
#pragma unroll
        for (int i = 0; i < 4; ++i) o[i] = fmaf(sc[i], d[i], o[i]);
      }
    }
    {
      u64* rec = p.ws + PG_OFF + (((size_t)(hkv * 4) * NB + bb) * 4 + sp) * PREC;
#pragma unroll
      for (int h = 0; h < 4; ++h) {
        const u32 mine = f2bf(o[h]);
        const u32 other = dppu<0xB1>(mine);
        if (lane < 16 && (lane & 1) == 0) put_gran(rec + (size_t)h * NB * 4 * PREC + 8 * wave + (lane >> 1), mine | (other << 16));
      }
      if (wave == 0 && lane < 4) {
        put_gran(rec + (size_t)lane * NB * 4 * PREC + 64, __float_as_uint(Mx));
        put_gran(rec + (size_t)lane * NB * 4 * PREC + 65, __float_as_uint(Lx));
      }
    }
    if (lane == 0) __hip_atomic_fetch_add((u32*)&ctrl[2], 1u, __ATOMIC_RELAXED, __HIP_MEMORY_SCOPE_WORKGROUP);
    if (wave == 0) STAMP(5);
    return;
  }

  const int st = tid - 512, sw = wave - 8;
  if (sw == 0) STAMP(6);
  if (st < 16) ctrl[st] = 0u;
  const int qcb = 8 * xg + (y & 7), qks = y >> 3;
  const bool has_ffn = (bid & 1) == 0 && (bid >> 1) < 112;
  const int fu = bid >> 1, fcb = fu % 28, fks = fu / 28;

  const int lb = st >> 5, lseg = st & 31;
  const u32x4 xa = *(const u32x4*)(p.x + lb * DM + lseg * 16);
  const u32x4 xb = *(const u32x4*)(p.x + lb * DM + lseg * 16 + 8);
#pragma unroll
  for (int i = 0; i < 4; ++i) {
    const int r = 32 * i + 8 * sw + (lane >> 3);
    dma16(p.wq + (size_t)(128 * qks + r) * 4096 + 64 * qcb + 8 * (lane & 7), smem + L_WQ + (32 * i + 8 * sw) * 128);
  }
  if (has_ffn) {
#pragma unroll
    for (int i = 0; i < 4; ++i) {
      const int r = 32 * i + 8 * sw + (lane >> 3);
      dma16(p.w1 + (size_t)(128 * fks + r) * FF + 64 * fcb + 8 * (lane & 7), smem + L_W1 + (32 * i + 8 * sw) * 128);
    }
  }
  lds_wait();
  __builtin_amdgcn_s_barrier();

  {
    float xv[16];
#pragma unroll
    for (int k = 0; k < 4; ++k) {
      xv[2 * k] = __uint_as_float(xa[k] << 16); xv[2 * k + 1] = __uint_as_float(xa[k] & 0xffff0000u);
      xv[8 + 2 * k] = __uint_as_float(xb[k] << 16); xv[8 + 2 * k + 1] = __uint_as_float(xb[k] & 0xffff0000u);
    }
    float s = 0.f;
#pragma unroll
    for (int k = 0; k < 16; ++k) s += xv[k];
#pragma unroll
    for (int o = 1; o < 32; o <<= 1) s += __shfl_xor(s, o);
    const float mean = s * (1.f / 512.f);
    float v = 0.f;
#pragma unroll
    for (int k = 0; k < 16; ++k) { const float d = xv[k] - mean; v = fmaf(d, d, v); }
#pragma unroll
    for (int o = 1; o < 32; o <<= 1) v += __shfl_xor(v, o);
    const float mb = bfr(mean), vb = bfr(v * (1.f / 512.f));
    const float den = bfr(sqrtf(bfr(vb + 1e-5f)));
    u32 o[8];
#pragma unroll
    for (int k = 0; k < 8; ++k) {
      const float a = bfr(xv[2 * k] - mb) / den, b = bfr(xv[2 * k + 1] - mb) / den;
      o[k] = f2bf(a) | (f2bf(b) << 16);
    }
    u32* dst = (u32*)(smem + L_XN + (lb * XN_STRIDE + lseg * 16) * 2);
    *(u32x4*)dst = u32x4{o[0], o[1], o[2], o[3]};
    *(u32x4*)(dst + 4) = u32x4{o[4], o[5], o[6], o[7]};
  }
  vm_wait0();
  lds_arrive(&ctrl[4], lane);
  lds_spin(&ctrl[4], 4);
  if (sw == 0) STAMP(7);
  {
    f32x4 d = {0.f, 0.f, 0.f, 0.f};
#pragma unroll
    for (int ks = 0; ks < 4; ++ks) {
      bf16x8 a = {0, 0, 0, 0, 0, 0, 0, 0};
      if (m16 < 8) a = *(const bf16x8*)(smem + L_XN + (m16 * XN_STRIDE + 128 * qks + ks * 32 + g4 * 8) * 2);
      d = __builtin_amdgcn_mfma_f32_16x16x32_bf16(a, bfrag_tr(smem + L_WQ, ks, sw, lane), d, 0, 0, 0);
    }
    if (lane < 32) {
#pragma unroll
      for (int i = 0; i < 4; ++i)
        put_gran(p.ws + QG_OFF + ((size_t)(4 * g4 + i) * 4 + qks) * 4096 + 64 * qcb + 16 * sw + m16, __float_as_uint(d[i]));
    }
    if (has_ffn) {
      f32x4 e = {0.f, 0.f, 0.f, 0.f};
#pragma unroll
      for (int ks = 0; ks < 4; ++ks) {
        bf16x8 a = {0, 0, 0, 0, 0, 0, 0, 0};
        if (m16 < 8) a = *(const bf16x8*)(smem + L_XN + (m16 * XN_STRIDE + 128 * fks + ks * 32 + g4 * 8) * 2);
        e = __builtin_amdgcn_mfma_f32_16x16x32_bf16(a, bfrag_tr(smem + L_W1, ks, sw, lane), e, 0, 0, 0);
      }
      if (lane < 32) {
#pragma unroll
        for (int i = 0; i < 4; ++i)
          put_gran(p.ws + HG_OFF + ((size_t)(4 * g4 + i) * 4 + fks) * FF + 64 * fcb + 16 * sw + m16, __float_as_uint(e[i]));
      }
    }
  }
  if (sw == 0) STAMP(8);
  {
    const u64* g = p.ws + QG_OFF + (size_t)bb * 4 * 4096 + hkv * 512 + 2 * st;
    u64 v[8];
    for (u32 it = 0; it < SPIN_CAP; ++it) {
      bool ok = true;
#pragma unroll
      for (int k = 0; k < 4; ++k) { v[2 * k] = get_gran(g + k * 4096); v[2 * k + 1] = get_gran(g + k * 4096 + 1); }
#pragma unroll
      for (int k = 0; k < 8; ++k) ok = ok && tag_ok(v[k]);
      if (__all(ok)) break;
      __builtin_amdgcn_s_sleep(2);
    }
    float q0 = 0.f, q1 = 0.f;
#pragma unroll
    for (int k = 0; k < 4; ++k) { q0 += __uint_as_float((u32)v[2 * k]); q1 += __uint_as_float((u32)v[2 * k + 1]); }
    ((u32*)(smem + L_Q))[st] = f2bf(q0) | (f2bf(q1) << 16);
    lds_arrive(&ctrl[0], lane);
  }
  if (sw == 0) STAMP(9);
#pragma unroll
  for (int i = 0; i < 6; ++i) {
    const int r0 = 32 * i + 8 * sw;
    if (r0 < 184) {
      const int r = r0 + (lane >> 3);
      const u16* src = r0 < 128 ? (p.wo + (size_t)(128 * h3 + r) * DM) : (p.w2 + (size_t)(56 * h3 + r - 128) * DM);
      dma16(src + 64 * cg + 8 * (lane & 7), smem + L_W3 + r0 * 128);
    }
  }
  ((u32*)(smem + L_W3 + 184 * 128))[st] = 0u;
  if (st < 32) ((u32*)(smem + L_A3))[(st >> 2) * (K3_STRIDE / 2) + 92 + (st & 3)] = 0u;
  const int ocol = 64 * cg + 2 * h3;
  u32 xx = 0, bb2 = 0;
  if (st < 8) { xx = *(const u32*)(p.x + st * DM + ocol); bb2 = *(const u32*)(p.b2 + ocol); }
  if (sw == 0) STAMP(10);
  if (st < 224) {
    const int b = st / 28, j = st - 28 * b;
    const u64* g = p.ws + HG_OFF + (size_t)b * 4 * FF + 56 * h3 + 2 * j;
    const u32 bias = *(const u32*)(p.b1 + 56 * h3 + 2 * j);
    u64 v[8];
    for (u32 it = 0; it < SPIN_CAP; ++it) {
      bool ok = true;
#pragma unroll
      for (int k = 0; k < 4; ++k) { v[2 * k] = get_gran(g + k * FF); v[2 * k + 1] = get_gran(g + k * FF + 1); }
#pragma unroll
      for (int k = 0; k < 8; ++k) ok = ok && tag_ok(v[k]);
      if (ok) break;
      __builtin_amdgcn_s_sleep(2);
    }
    float h0 = 0.f, h1 = 0.f;
#pragma unroll
    for (int k = 0; k < 4; ++k) { h0 += __uint_as_float((u32)v[2 * k]); h1 += __uint_as_float((u32)v[2 * k + 1]); }
    h0 = gelu_tanh(bfr(bfr(h0) + bf2f(bias & 0xffffu)));
    h1 = gelu_tanh(bfr(bfr(h1) + bf2f(bias >> 16)));
    ((u32*)(smem + L_A3))[b * (K3_STRIDE / 2) + 64 + j] = f2bf(h0) | (f2bf(h1) << 16);
  }
  if (sw == 0) STAMP(11);
  lds_spin(&ctrl[2], 8);
  if (sw == 0) STAMP(12);
  {
    const int b = st >> 5, j = st & 31;
    const u64* base = p.ws + PG_OFF + ((size_t)h3 * NB + b) * 4 * PREC;
    u64 ov[8], mv[4], lv[4];
    for (u32 it = 0; it < SPIN_CAP; ++it) {
      bool ok = true;
#pragma unroll
      for (int s = 0; s < 4; ++s) {
        ov[2 * s] = get_gran(base + s * PREC + 2 * j);
        ov[2 * s + 1] = get_gran(base + s * PREC + 2 * j + 1);
        mv[s] = get_gran(base + s * PREC + 64);
        lv[s] = get_gran(base + s * PREC + 65);
      }
#pragma unroll
      for (int s = 0; s < 4; ++s) ok = ok && tag_ok(ov[2 * s]) && tag_ok(ov[2 * s + 1]) && tag_ok(mv[s]) && tag_ok(lv[s]);
      if (__all(ok)) break;
      __builtin_amdgcn_s_sleep(1);
    }
    float M = -INFINITY;
#pragma unroll
    for (int s = 0; s < 4; ++s) M = fmaxf(M, __uint_as_float((u32)mv[s]));
    float L = 0.f, o[4] = {0.f, 0.f, 0.f, 0.f};
#pragma unroll
    for (int s = 0; s < 4; ++s) {
      const float wt = __builtin_amdgcn_exp2f((__uint_as_float((u32)mv[s]) - M) * LOG2E);
      L = fmaf(wt, __uint_as_float((u32)lv[s]), L);
      o[0] = fmaf(wt, bf2f((u32)ov[2 * s] & 0xffffu), o[0]);
      o[1] = fmaf(wt, bf2f(((u32)ov[2 * s]) >> 16), o[1]);
      o[2] = fmaf(wt, bf2f((u32)ov[2 * s + 1] & 0xffffu), o[2]);
      o[3] = fmaf(wt, bf2f(((u32)ov[2 * s + 1]) >> 16), o[3]);
    }
    const float inv = 1.f / L;
    u32* a3 = (u32*)(smem + L_A3) + b * (K3_STRIDE / 2) + 2 * j;
    a3[0] = f2bf(o[0] * inv) | (f2bf(o[1] * inv) << 16);
    a3[1] = f2bf(o[2] * inv) | (f2bf(o[3] * inv) << 16);
  }
  if (sw == 0) STAMP(13);
  vm_wait0();
  lds_arrive(&ctrl[5], lane);
  lds_spin(&ctrl[5], 4);
  {
    f32x4 d = {0.f, 0.f, 0.f, 0.f};
#pragma unroll
    for (int ks = 0; ks < 6; ++ks) {
      bf16x8 a = {0, 0, 0, 0, 0, 0, 0, 0};
      if (m16 < 8) a = *(const bf16x8*)(smem + L_A3 + (m16 * K3_STRIDE + ks * 32 + g4 * 8) * 2);
      d = __builtin_amdgcn_mfma_f32_16x16x32_bf16(a, bfrag_tr(smem + L_W3, ks, sw, lane), d, 0, 0, 0);
    }
    if (lane < 32) {
      const int col = 16 * sw + m16;
      u64* og = p.ws + OG_OFF + ((((size_t)cg * 32 + (col >> 1)) * 32 + h3) * 8) * 2 + (col & 1);
#pragma unroll
      for (int i = 0; i < 4; ++i) put_gran(og + (4 * g4 + i) * 2, __float_as_uint(d[i]));
    }
  }
  if (sw == 0) STAMP(14);
  {
    const u64* base = p.ws + OG_OFF + (((size_t)cg * 32 + h3) * 32) * 16 + 2 * st;
    u64 a = 0, b = 0;
    for (u32 it = 0; it < SPIN_CAP; ++it) {
      a = get_gran(base); b = get_gran(base + 1);
      if (__all(tag_ok(a) && tag_ok(b))) break;
      __builtin_amdgcn_s_sleep(1);
    }
    if (sw == 0) STAMP(15);
    float s0 = __uint_as_float((u32)a), s1 = __uint_as_float((u32)b);
    s0 += dpp<0x128>(s0); s1 += dpp<0x128>(s1);
    s0 = x32_sum(x16_sum(s0)); s1 = x32_sum(x16_sum(s1));
    float* red = (float*)(smem + L_RED);
    if (lane < 8) { red[sw * 16 + lane * 2] = s0; red[sw * 16 + lane * 2 + 1] = s1; }
    lds_arrive(&ctrl[6], lane);
    if (sw != 0) return;
    lds_spin(&ctrl[6], 4);
    if (lane < 8) {
      float r0 = 0.f, r1 = 0.f;
#pragma unroll
      for (int w = 0; w < 4; ++w) { r0 += red[w * 16 + lane * 2]; r1 += red[w * 16 + lane * 2 + 1]; }
      r0 += bf2f(xx & 0xffffu) + bf2f(bb2 & 0xffffu);
      r1 += bf2f(xx >> 16) + bf2f(bb2 >> 16);
      *(u32*)(p.out + lane * DM + ocol) = f2bf(r0) | (f2bf(r1) << 16);
    }
    STAMP(16);
  }
}

}

extern "C" void kernel_launch(void* const* d_in, const int* in_sizes, int n_in,
                              void* d_out, int out_size, void* d_ws, size_t ws_size,
                              hipStream_t stream) {
  Params p;
  p.x = (const u16*)d_in[0];
  p.kc = (const u16*)d_in[1];
  p.vc = (const u16*)d_in[2];
  p.wq = (const u16*)d_in[3];
  p.wo = (const u16*)d_in[4];
  p.w1 = (const u16*)d_in[6];
  p.b1 = (const u16*)d_in[7];
  p.w2 = (const u16*)d_in[8];
  p.b2 = (const u16*)d_in[9];
  p.out = (u16*)d_out;
  p.ws = (u64*)d_ws;
  (void)hipMemsetAsync(d_ws, 0, WS_BYTES, stream);
  hipLaunchKernelGGL(fused_decode, dim3(NWG), dim3(NTHR), 0, stream, p);
}
```

```cpp
#include <hip/hip_runtime.h>
#include <hip/hip_bf16.h>
#include <stdint.h>

typedef unsigned int u32;
typedef unsigned long long u64;
typedef unsigned short u16;
typedef u32 u32x4 __attribute__((ext_vector_type(4)));
typedef float f32x4 __attribute__((ext_vector_type(4)));
typedef short bf16x8 __attribute__((ext_vector_type(8)));
typedef short s16x4 __attribute__((ext_vector_type(4)));

#define GAS __attribute__((address_space(1)))
#define LAS __attribute__((address_space(3)))
#define RLX_AGENT __ATOMIC_RELAXED, __HIP_MEMORY_SCOPE_AGENT

namespace {

constexpr int NB = 8, DM = 512, HQ = 32, HKV = 8, SK = 4096, DH = 128, WINDOW = 1024, FF = 1792;
constexpr int KOFF = SK - WINDOW;
constexpr int NWG = 256, NTHR = 768;
constexpr u32 TAG = 0x3C5A0001u;
constexpr u32 SPIN_CAP = 1u << 22;
constexpr float LOG2E = 1.4426950408889634f;

constexpr int PREC = 66;
constexpr size_t QG_OFF = 0, QG_N = (size_t)NB * 2 * 4096;
constexpr size_t HG_OFF = QG_OFF + QG_N, HG_N = (size_t)NB * 4 * FF;
constexpr size_t PG_OFF = HG_OFF + HG_N, PG_N = (size_t)HQ * NB * 2 * PREC;
constexpr size_t OG_OFF = PG_OFF + PG_N, OG_N = (size_t)8 * 16 * 16 * 8 * 4;
constexpr size_t WS_GRAN = OG_OFF + OG_N;
constexpr size_t WS_BYTES = WS_GRAN * 8;
static_assert(WS_BYTES % 16 == 0, "memset block must be a multiple of 16 bytes");

constexpr int L_CTRL = 0;
constexpr int L_Q = 64;
constexpr int L_ML = L_Q + 1024;
constexpr int L_SC = L_ML + 256;
constexpr int L_PB = L_SC + 1024;
constexpr int L_V = L_PB + 16384;
constexpr int LA_END = L_V + 131072;
constexpr int XN_STRIDE = 520;
constexpr int K3_STRIDE = 392;
constexpr int L_XN = 64;
constexpr int L_WQ = L_XN + NB * XN_STRIDE * 2;
constexpr int L_W1 = L_WQ + 32768;
constexpr int L_W3 = L_W1 + 16384;
constexpr int L_A3 = L_W3 + 49152;
constexpr int L_RED = L_A3 + NB * K3_STRIDE * 2;
constexpr int LB_END = L_RED + 1024;
constexpr int LDS_BYTES = LA_END > LB_END ? LA_END : LB_END;
static_assert(LDS_BYTES > 80 * 1024 && LDS_BYTES <= 160 * 1024, "one workgroup per CU");
static_assert(L_WQ % 16 == 0 && L_W3 % 16 == 0 && L_A3 % 16 == 0 && L_V % 16 == 0 && L_PB % 16 == 0 && L_RED % 16 == 0, "align");

struct Params {
  const u16* x; const u16* kc; const u16* vc; const u16* wq; const u16* wo;
  const u16* w1; const u16* b1; const u16* w2; const u16* b2;
  u16* out; u64* ws;
};

__device__ __forceinline__ float bf2f(u32 h) { return __uint_as_float(h << 16); }
__device__ __forceinline__ u32 f2bf(float f) {
  __bf16 h = (__bf16)f;
  return (u32)__builtin_bit_cast(u16, h);
}
__device__ __forceinline__ float bfr(float f) { return bf2f(f2bf(f)); }

template <int CTRL> __device__ __forceinline__ float dpp(float x) {
  return __builtin_bit_cast(float, __builtin_amdgcn_mov_dpp(__builtin_bit_cast(int, x), CTRL, 0xf, 0xf, true));
}
template <int CTRL> __device__ __forceinline__ u32 dppu(u32 x) {
  return (u32)__builtin_amdgcn_mov_dpp((int)x, CTRL, 0xf, 0xf, true);
}
__device__ __forceinline__ float x16_max(float x) {
  auto s = __builtin_amdgcn_permlane16_swap(__float_as_uint(x), __float_as_uint(x), false, false);
  return fmaxf(__uint_as_float(s[0]), __uint_as_float(s[1]));
}
__device__ __forceinline__ float x32_max(float x) {
  auto t = __builtin_amdgcn_permlane32_swap(__float_as_uint(x), __float_as_uint(x), false, false);
  return fmaxf(__uint_as_float(t[0]), __uint_as_float(t[1]));
}
__device__ __forceinline__ float x16_sum(float x) {
  auto s = __builtin_amdgcn_permlane16_swap(__float_as_uint(x), __float_as_uint(x), false, false);
  return __uint_as_float(s[0]) + __uint_as_float(s[1]);
}
__device__ __forceinline__ float x32_sum(float x) {
  auto t = __builtin_amdgcn_permlane32_swap(__float_as_uint(x), __float_as_uint(x), false, false);
  return __uint_as_float(t[0]) + __uint_as_float(t[1]);
}

__device__ __forceinline__ void put_gran(u64* g, u32 v) {
  __hip_atomic_store((GAS u64*)g, ((u64)TAG << 32) | (u64)v, RLX_AGENT);
}
__device__ __forceinline__ u64 get_gran(const u64* g) {
  return __hip_atomic_load((GAS u64*)g, RLX_AGENT);
}
__device__ __forceinline__ bool tag_ok(u64 v) { return (u32)(v >> 32) == TAG; }

__device__ __forceinline__ void lds_wait() { asm volatile("s_waitcnt lgkmcnt(0)" ::: "memory"); }
__device__ __forceinline__ void vm_wait0() { asm volatile("s_waitcnt vmcnt(0)" ::: "memory"); }

typedef volatile LAS u32 lctr;
__device__ __forceinline__ void lds_add1(lctr* c) {
  __hip_atomic_fetch_add((LAS u32*)c, 1u, __ATOMIC_RELAXED, __HIP_MEMORY_SCOPE_WORKGROUP);
}
__device__ __forceinline__ void lds_arrive(lctr* c, int lane) {
  lds_wait();
  if (lane == 0) lds_add1(c);
  asm volatile("" ::: "memory");
}
__device__ __forceinline__ void lds_spin(lctr* c, u32 target) {
  for (u32 it = 0; it < SPIN_CAP; ++it) {
    if (*c >= target) break;
    __builtin_amdgcn_s_sleep(1);
  }
  asm volatile("" ::: "memory");
}

__device__ __forceinline__ float gelu_tanh(float v) {
  const float u = 0.7978845608028654f * (v + 0.044715f * v * v * v);
  return 0.5f * v * (1.f + tanhf(u));
}

__device__ __forceinline__ void dma16(const void* gsrc, unsigned char* lds_base) {
  __builtin_amdgcn_global_load_lds(gsrc, (LAS void*)lds_base, 16, 0, 2);
}
__device__ __forceinline__ bf16x8 bfrag_tr(const unsigned char* tile, int ks, int nt, int lane) {
  const int g = lane >> 4, q = (lane & 15) >> 2, pp = lane & 3;
  const unsigned char* a = tile + (32 * ks + 8 * g + q) * 128 + (16 * nt + 4 * pp) * 2;
  const s16x4 lo = __builtin_amdgcn_ds_read_tr16_b64_v4i16((LAS s16x4*)a);
  const s16x4 hi = __builtin_amdgcn_ds_read_tr16_b64_v4i16((LAS s16x4*)(a + 4 * 128));
  return bf16x8{lo[0], lo[1], lo[2], lo[3], hi[0], hi[1], hi[2], hi[3]};
}
__device__ __forceinline__ int vswz(int row) { return ((row & 3) << 2) | ((row >> 2) & 3); }

#define KV_WIN 8
#define PG_SLEEP 4
#define STAMP(k) do { } while (0)

__global__ void __launch_bounds__(NTHR, 3) fused_decode(Params p) {
  __shared__ __attribute__((aligned(16))) unsigned char smem[LDS_BYTES];
  lctr* ctrl = (lctr*)(smem + L_CTRL);

  const int tid = threadIdx.x, lane = tid & 63;
  const int wave = __builtin_amdgcn_readfirstlane(tid >> 6);
  const int bid = blockIdx.x, xg = bid & 7, y = bid >> 3;
  const int g4 = lane >> 4, m16 = lane & 15;

  if (y < 16) {
    const int hkv = xg, bb = y >> 1, sp = y & 1;
    if (wave >= 8) {
      const int st = tid - 512;
      if (st < 16) ctrl[st] = 0u;
      lds_wait();
      __builtin_amdgcn_s_barrier();
      if (wave == 8) STAMP(6);
      const u64* g = p.ws + QG_OFF + (size_t)bb * 2 * 4096 + hkv * 512 + 2 * st;
      u64 v[4];
      for (u32 it = 0; it < SPIN_CAP; ++it) {
        v[0] = get_gran(g); v[1] = get_gran(g + 1); v[2] = get_gran(g + 4096); v[3] = get_gran(g + 4097);
        if (__all(tag_ok(v[0]) && tag_ok(v[1]) && tag_ok(v[2]) && tag_ok(v[3]))) break;
        __builtin_amdgcn_s_sleep(8);
      }
      const float q0 = __uint_as_float((u32)v[0]) + __uint_as_float((u32)v[2]);
      const float q1 = __uint_as_float((u32)v[1]) + __uint_as_float((u32)v[3]);
      ((u32*)(smem + L_Q))[st] = f2bf(q0) | (f2bf(q1) << 16);
      lds_arrive(&ctrl[0], lane);
      if (wave == 8) STAMP(7);
      return;
    }
    if (wave == 0) STAMP(0);
    const size_t kvbase = ((size_t)(bb * HKV + hkv) * SK + KOFF + sp * 512 + wave * 64) * DH;
    const u32x4* kp = (const u32x4*)(p.kc + kvbase + (size_t)m16 * DH + g4 * 8);
    u32x4 kf[16];
#define KV_ISSUE(n) do { \
      if ((n) < 16) { kf[(n) & 15] = __builtin_nontemporal_load(kp + ((n) >> 2) * 16 * (DH / 8) + ((n) & 3) * 4); } \
      else { const int row_ = 64 * wave + 4 * ((n) - 16) + g4; \
             dma16(p.vc + kvbase + (size_t)(4 * ((n) - 16) + g4) * DH + ((m16 ^ vswz(row_)) * 8), smem + L_V + (64 * wave + 4 * ((n) - 16)) * 256); } \
      asm volatile("" ::: "memory"); } while (0)
#pragma unroll
    for (int n = 0; n < KV_WIN; ++n) KV_ISSUE(n);
    __builtin_amdgcn_s_barrier();
#pragma unroll
    for (int n = KV_WIN; n < 32; ++n) {
      asm volatile("s_waitcnt vmcnt(%0)" :: "i"(KV_WIN - 1) : "memory");
      KV_ISSUE(n);
    }
#undef KV_ISSUE
    if (wave == 0) STAMP(1);
    lds_spin(&ctrl[0], 4);
    if (wave == 0) STAMP(2);
    bf16x8 qf[4];
#pragma unroll
    for (int t = 0; t < 4; ++t) {
      bf16x8 z = {0, 0, 0, 0, 0, 0, 0, 0};
      if (m16 < 4) z = *(const bf16x8*)(smem + L_Q + (m16 * 128 + t * 32 + g4 * 8) * 2);
      qf[t] = z;
    }
    constexpr float SCALE = 0.08838834764831845f;
    float a[16];
#pragma unroll
    for (int T = 0; T < 4; ++T) {
      f32x4 s = {0.f, 0.f, 0.f, 0.f};
#pragma unroll
      for (int t = 0; t < 4; ++t) s = __builtin_amdgcn_mfma_f32_16x16x32_bf16(__builtin_bit_cast(bf16x8, kf[4 * T + t]), qf[t], s, 0, 0, 0);
#pragma unroll
      for (int i = 0; i < 4; ++i) a[4 * T + i] = bfr(bfr(s[i]) * SCALE);
    }
    float m = a[0];
#pragma unroll
    for (int i = 1; i < 16; ++i) m = fmaxf(m, a[i]);
    m = x32_max(x16_max(m));
    float l = 0.f;
    u32x4 pf[2];
#pragma unroll
    for (int i = 0; i < 8; ++i) {
      const u32 lo = f2bf(__builtin_amdgcn_exp2f((a[2 * i] - m) * LOG2E)), hi = f2bf(__builtin_amdgcn_exp2f((a[2 * i + 1] - m) * LOG2E));
      pf[i >> 2][i & 3] = lo | (hi << 16);
      l += bf2f(lo) + bf2f(hi);
    }
    l = x32_sum(x16_sum(l));
    *(u32x4*)(smem + L_PB + ((wave * 2 + 0) * 64 + lane) * 16) = pf[0];
    *(u32x4*)(smem + L_PB + ((wave * 2 + 1) * 64 + lane) * 16) = pf[1];
    if (lane < 4) {
      float* ml = (float*)(smem + L_ML) + (wave * 4 + lane) * 2;
      ml[0] = m; ml[1] = l;
    }
    if (wave == 0) STAMP(3);
    vm_wait0();
    lds_arrive(&ctrl[1], lane);
    lds_spin(&ctrl[1], 8);
    if (wave == 0) STAMP(4);
    float Mx, Lx;
    {
      const int ws = (lane >> 2) & 7, h = lane & 3;
      const float* ml = (const float*)(smem + L_ML) + (ws * 4 + h) * 2;
      const float mw = ml[0], lw = ml[1];
      float M = mw;
      M = fmaxf(M, dpp<0x124>(M)); M = fmaxf(M, dpp<0x128>(M)); M = x16_max(M);
      const float sc = __builtin_amdgcn_exp2f((mw - M) * LOG2E);
      float L = sc * lw;
      L += dpp<0x124>(L); L += dpp<0x128>(L); L = x16_sum(L);
      Mx = M; Lx = L;
      if (lane < 32) ((float*)(smem + L_SC))[wave * 32 + lane] = sc;
      lds_wait();
      __builtin_amdgcn_wave_barrier();
    }
    f32x4 o = {0.f, 0.f, 0.f, 0.f};
    {
      const int q = m16 >> 2, pp = lane & 3;
      const int ch = 2 * wave + (pp >> 1);
#pragma unroll
      for (int w = 0; w < 8; ++w) {
        f32x4 d = {0.f, 0.f, 0.f, 0.f};
#pragma unroll
        for (int c = 0; c < 2; ++c) {
          const bf16x8 pa = *(const bf16x8*)(smem + L_PB + ((w * 2 + c) * 64 + lane) * 16);
          const int r0 = 64 * w + 32 * c + 4 * g4 + q, r1 = r0 + 16;
          const s16x4 lo = __builtin_amdgcn_ds_read_tr16_b64_v4i16((LAS s16x4*)(smem + L_V + r0 * 256 + ((ch ^ vswz(r0)) * 16) + 8 * (pp & 1)));
          const s16x4 hi = __builtin_amdgcn_ds_read_tr16_b64_v4i16((LAS s16x4*)(smem + L_V + r1 * 256 + ((ch ^ vswz(r1)) * 16) + 8 * (pp & 1)));
          const bf16x8 vb = {lo[0], lo[1], lo[2], lo[3], hi[0], hi[1], hi[2], hi[3]};
          d = __builtin_amdgcn_mfma_f32_16x16x32_bf16(pa, vb, d, 0, 0, 0);
        }
        const f32x4 sc = *(const f32x4*)(smem + L_SC + (wave * 32 + w * 4) * 4);
#pragma unroll
        for (int i = 0; i < 4; ++i) o[i] = fmaf(sc[i], d[i], o[i]);
      }
    }
    {
      u64* rec = p.ws + PG_OFF + (((size_t)(hkv * 4) * NB + bb) * 2 + sp) * PREC;
#pragma unroll
      for (int h = 0; h < 4; ++h) {
        const u32 mine = f2bf(o[h]);
        const u32 other = dppu<0xB1>(mine);
        if (lane < 16 && (lane & 1) == 0) put_gran(rec + (size_t)h * NB * 2 * PREC + 8 * wave + (lane >> 1), mine | (other << 16));
      }
      if (wave == 0 && lane < 4) {
        put_gran(rec + (size_t)lane * NB * 2 * PREC + 64, __float_as_uint(Mx));
        put_gran(rec + (size_t)lane * NB * 2 * PREC + 65, __float_as_uint(Lx));
      }
    }
    if (wave == 0) STAMP(5);
    return;
  }

  if (wave >= 8) return;
  const int j = xg + 8 * (y - 16);
  if (wave == 0) STAMP(8);
  if (tid < 16) ctrl[tid] = 0u;
  const int qcb = j & 63, qks = j >> 6;
  const bool has_ffn = j < 112;
  const int fcb = j % 28, fks = j / 28;
  const int rg = j >> 3, cg = j & 7;

  const u32x4 xa = *(const u32x4*)(p.x + wave * DM + lane * 8);
#pragma unroll
  for (int i = 0; i < 4; ++i) {
    const int r0 = 64 * i + 8 * wave;
    dma16(p.wq + (size_t)(256 * qks + r0 + (lane >> 3)) * 4096 + 64 * qcb + 8 * (lane & 7), smem + L_WQ + r0 * 128);
  }
  if (has_ffn) {
#pragma unroll
    for (int i = 0; i < 2; ++i) {
      const int r0 = 64 * i + 8 * wave;
      dma16(p.w1 + (size_t)(128 * fks + r0 + (lane >> 3)) * FF + 64 * fcb + 8 * (lane & 7), smem + L_W1 + r0 * 128);
    }
  }
  {
    float xv[8];
#pragma unroll
    for (int k = 0; k < 4; ++k) { xv[2 * k] = __uint_as_float(xa[k] << 16); xv[2 * k + 1] = __uint_as_float(xa[k] & 0xffff0000u); }
    float s = 0.f;
#pragma unroll
    for (int k = 0; k < 8; ++k) s += xv[k];
    s += dpp<0x121>(s); s += dpp<0x122>(s); s += dpp<0x124>(s); s += dpp<0x128>(s); s = x32_sum(x16_sum(s));
    const float mean = s * (1.f / 512.f);
    float v = 0.f;
#pragma unroll
    for (int k = 0; k < 8; ++k) { const float d = xv[k] - mean; v = fmaf(d, d, v); }
    v += dpp<0x121>(v); v += dpp<0x122>(v); v += dpp<0x124>(v); v += dpp<0x128>(v); v = x32_sum(x16_sum(v));
    const float mb = bfr(mean), vb = bfr(v * (1.f / 512.f));
    const float den = bfr(sqrtf(bfr(vb + 1e-5f)));
    u32x4 o;
#pragma unroll
    for (int k = 0; k < 4; ++k) {
      const float a = bfr(xv[2 * k] - mb) / den, b = bfr(xv[2 * k + 1] - mb) / den;
      o[k] = f2bf(a) | (f2bf(b) << 16);
    }
    *(u32x4*)(smem + L_XN + (wave * XN_STRIDE + lane * 8) * 2) = o;
  }
  vm_wait0();
  lds_wait();
  __builtin_amdgcn_s_barrier();
  if (wave == 0) STAMP(9);
  if (wave < 4) {
    f32x4 d = {0.f, 0.f, 0.f, 0.f};
#pragma unroll
    for (int ks = 0; ks < 8; ++ks) {
      bf16x8 a = {0, 0, 0, 0, 0, 0, 0, 0};
      if (m16 < 8) a = *(const bf16x8*)(smem + L_XN + (m16 * XN_STRIDE + 256 * qks + ks * 32 + g4 * 8) * 2);
      d = __builtin_amdgcn_mfma_f32_16x16x32_bf16(a, bfrag_tr(smem + L_WQ, ks, wave, lane), d, 0, 0, 0);
    }
    if (lane < 32) {
#pragma unroll
      for (int i = 0; i < 4; ++i)
        put_gran(p.ws + QG_OFF + ((size_t)(4 * g4 + i) * 2 + qks) * 4096 + 64 * qcb + 16 * wave + m16, __float_as_uint(d[i]));
    }
  } else if (has_ffn) {
    f32x4 e = {0.f, 0.f, 0.f, 0.f};
#pragma unroll
    for (int ks = 0; ks < 4; ++ks) {
      bf16x8 a = {0, 0, 0, 0, 0, 0, 0, 0};
      if (m16 < 8) a = *(const bf16x8*)(smem + L_XN + (m16 * XN_STRIDE + 128 * fks + ks * 32 + g4 * 8) * 2);
      e = __builtin_amdgcn_mfma_f32_16x16x32_bf16(a, bfrag_tr(smem + L_W1, ks, wave - 4, lane), e, 0, 0, 0);
    }
    if (lane < 32) {
#pragma unroll
      for (int i = 0; i < 4; ++i)
        put_gran(p.ws + HG_OFF + ((size_t)(4 * g4 + i) * 4 + fks) * FF + 64 * fcb + 16 * (wave - 4) + m16, __float_as_uint(e[i]));
    }
  }
  if (wave == 0) STAMP(10);
#pragma unroll
  for (int i = 0; i < 6; ++i) {
    const int r0 = 64 * i + 8 * wave;
    if (r0 < 368) {
      const int r = r0 + (lane >> 3);
      const u16* src = r0 < 256 ? (p.wo + (size_t)(256 * rg + r) * DM) : (p.w2 + (size_t)(112 * rg + r - 256) * DM);
      dma16(src + 64 * cg + 8 * (lane & 7), smem + L_W3 + r0 * 128);
    }
  }
  ((u32*)(smem + L_W3 + 368 * 128))[tid] = 0u;
  if (tid < 64) ((u32*)(smem + L_A3))[(tid >> 3) * (K3_STRIDE / 2) + 184 + (tid & 7)] = 0u;
  const int ocol = 64 * cg + 4 * rg;
  float xres = 0.f;
  if (tid < 32) xres = bf2f(p.x[(tid >> 2) * DM + ocol + (tid & 3)]) + bf2f(p.b2[ocol + (tid & 3)]);
  if (tid < 448) {
    const int b = tid / 56, jj = tid - 56 * b;
    const u64* g = p.ws + HG_OFF + (size_t)b * 4 * FF + 112 * rg + 2 * jj;
    const u32 bias = *(const u32*)(p.b1 + 112 * rg + 2 * jj);
    u64 v[8];
    for (u32 it = 0; it < SPIN_CAP; ++it) {
      bool ok = true;
#pragma unroll
      for (int k = 0; k < 4; ++k) { v[2 * k] = get_gran(g + k * FF); v[2 * k + 1] = get_gran(g + k * FF + 1); }
#pragma unroll
      for (int k = 0; k < 8; ++k) ok = ok && tag_ok(v[k]);
      if (ok) break;
      __builtin_amdgcn_s_sleep(4);
    }
    float h0 = 0.f, h1 = 0.f;
#pragma unroll
    for (int k = 0; k < 4; ++k) { h0 += __uint_as_float((u32)v[2 * k]); h1 += __uint_as_float((u32)v[2 * k + 1]); }
    h0 = gelu_tanh(bfr(bfr(h0) + bf2f(bias & 0xffffu)));
    h1 = gelu_tanh(bfr(bfr(h1) + bf2f(bias >> 16)));
    ((u32*)(smem + L_A3))[b * (K3_STRIDE / 2) + 128 + jj] = f2bf(h0) | (f2bf(h1) << 16);
  }
  if (wave == 0) STAMP(11);
  {
    const int hh = tid >> 8, b = (tid >> 5) & 7, jj = tid & 31;
    const u64* base = p.ws + PG_OFF + ((size_t)(2 * rg + hh) * NB + b) * 2 * PREC;
    u64 ov[4], mv[2], lv[2];
    for (u32 it = 0; it < SPIN_CAP; ++it) {
      bool ok = true;
#pragma unroll
      for (int s = 0; s < 2; ++s) {
        ov[2 * s] = get_gran(base + s * PREC + 2 * jj);
        ov[2 * s + 1] = get_gran(base + s * PREC + 2 * jj + 1);
        mv[s] = get_gran(base + s * PREC + 64);
        lv[s] = get_gran(base + s * PREC + 65);
      }
#pragma unroll
      for (int s = 0; s < 2; ++s) ok = ok && tag_ok(ov[2 * s]) && tag_ok(ov[2 * s + 1]) && tag_ok(mv[s]) && tag_ok(lv[s]);
      if (__all(ok)) break;
      __builtin_amdgcn_s_sleep(PG_SLEEP);
    }
    const float m0 = __uint_as_float((u32)mv[0]), m1 = __uint_as_float((u32)mv[1]);
    const float M = fmaxf(m0, m1);
    const float w0 = __builtin_amdgcn_exp2f((m0 - M) * LOG2E), w1 = __builtin_amdgcn_exp2f((m1 - M) * LOG2E);
    const float inv = 1.f / fmaf(w0, __uint_as_float((u32)lv[0]), w1 * __uint_as_float((u32)lv[1]));
    const float c0 = w0 * inv, c1 = w1 * inv;
    float o[4];
    o[0] = c0 * bf2f((u32)ov[0] & 0xffffu) + c1 * bf2f((u32)ov[2] & 0xffffu);
    o[1] = c0 * bf2f(((u32)ov[0]) >> 16) + c1 * bf2f(((u32)ov[2]) >> 16);
    o[2] = c0 * bf2f((u32)ov[1] & 0xffffu) + c1 * bf2f((u32)ov[3] & 0xffffu);
    o[3] = c0 * bf2f(((u32)ov[1]) >> 16) + c1 * bf2f(((u32)ov[3]) >> 16);
    u32* a3 = (u32*)(smem + L_A3) + b * (K3_STRIDE / 2) + 64 * hh + 2 * jj;
    a3[0] = f2bf(o[0]) | (f2bf(o[1]) << 16);
    a3[1] = f2bf(o[2]) | (f2bf(o[3]) << 16);
  }
  if (wave == 0) STAMP(12);
  vm_wait0();
  lds_wait();
  __builtin_amdgcn_s_barrier();
  if (wave == 0) STAMP(13);
  if (wave < 4) {
    f32x4 d = {0.f, 0.f, 0.f, 0.f};
#pragma unroll
    for (int ks = 0; ks < 12; ++ks) {
      bf16x8 a = {0, 0, 0, 0, 0, 0, 0, 0};
      if (m16 < 8) a = *(const bf16x8*)(smem + L_A3 + (m16 * K3_STRIDE + ks * 32 + g4 * 8) * 2);
      d = __builtin_amdgcn_mfma_f32_16x16x32_bf16(a, bfrag_tr(smem + L_W3, ks, wave, lane), d, 0, 0, 0);
    }
    if (lane < 32) {
      const int col = 16 * wave + m16;
      u64* og = p.ws + OG_OFF + ((((size_t)cg * 16 + (col >> 2)) * 16 + rg) * 8) * 4 + (col & 3);
#pragma unroll
      for (int i = 0; i < 4; ++i) put_gran(og + (4 * g4 + i) * 4, __float_as_uint(d[i]));
    }
  }
  if (wave == 0) STAMP(14);
  {
    const u64* base = p.ws + OG_OFF + (((size_t)cg * 16 + rg) * 16) * 32 + tid;
    u64 a = 0;
    for (u32 it = 0; it < SPIN_CAP; ++it) {
      a = get_gran(base);
      if (__all(tag_ok(a))) break;
      __builtin_amdgcn_s_sleep(1);
    }
    if (wave == 0) STAMP(15);
    float s0 = x32_sum(__uint_as_float((u32)a));
    float* red = (float*)(smem + L_RED);
    if (lane < 32) red[wave * 32 + lane] = s0;
    lds_wait();
    __builtin_amdgcn_s_barrier();
    if (tid < 32) {
      float r = xres;
#pragma unroll
      for (int w = 0; w < 8; ++w) r += red[w * 32 + tid];
      p.out[(tid >> 2) * DM + ocol + (tid & 3)] = (u16)f2bf(r);
    }
    if (wave == 0) STAMP(16);
  }
}

}

extern "C" void kernel_launch(void* const* d_in, const int* in_sizes, int n_in,
                              void* d_out, int out_size, void* d_ws, size_t ws_size,
                              hipStream_t stream) {
  Params p;
  p.x = (const u16*)d_in[0];
  p.kc = (const u16*)d_in[1];
  p.vc = (const u16*)d_in[2];
  p.wq = (const u16*)d_in[3];
  p.wo = (const u16*)d_in[4];
  p.w1 = (const u16*)d_in[6];
  p.b1 = (const u16*)d_in[7];
  p.w2 = (const u16*)d_in[8];
  p.b2 = (const u16*)d_in[9];
  p.out = (u16*)d_out;
  p.ws = (u64*)d_ws;
  (void)hipMemsetAsync(d_ws, 0, WS_BYTES, stream);
  hipLaunchKernelGGL(fused_decode, dim3(NWG), dim3(NTHR), 0, stream, p);
}
```

```cpp
#include <hip/hip_runtime.h>
#include <hip/hip_bf16.h>
#include <stdint.h>

typedef unsigned int u32;
typedef unsigned long long u64;
typedef unsigned short u16;
typedef u32 u32x4 __attribute__((ext_vector_type(4)));
typedef float f32x4 __attribute__((ext_vector_type(4)));
typedef short bf16x8 __attribute__((ext_vector_type(8)));
typedef short s16x4 __attribute__((ext_vector_type(4)));

#define GAS __attribute__((address_space(1)))
#define LAS __attribute__((address_space(3)))
#define RLX_AGENT __ATOMIC_RELAXED, __HIP_MEMORY_SCOPE_AGENT

namespace {

constexpr int NB = 8, DM = 512, HQ = 32, HKV = 8, SK = 4096, DH = 128, WINDOW = 1024, FF = 1792;
constexpr int KOFF = SK - WINDOW;
constexpr int NWG = 256, NTHR = 1024;
constexpr u32 TAG = 0x3C5A0001u;
constexpr u32 SPIN_CAP = 1u << 22;
constexpr float LOG2E = 1.4426950408889634f;

constexpr int NSP = 3;
constexpr int PREC = 66;
constexpr size_t QG_OFF = 0, QG_N = (size_t)NB * 2048;
constexpr size_t HG_OFF = QG_OFF + QG_N, HG_N = (size_t)NB * 2 * FF;
constexpr size_t PG_OFF = HG_OFF + HG_N, PG_N = (size_t)HQ * NB * NSP * PREC;
constexpr size_t OG_OFF = PG_OFF + PG_N, OG_N = (size_t)8 * 8 * 8 * 8 * 8;
constexpr size_t WS_GRAN = OG_OFF + OG_N;
constexpr size_t WS_BYTES = WS_GRAN * 8;
static_assert(WS_BYTES % 16 == 0, "memset block must be a multiple of 16 bytes");

constexpr int L_CTRL = 0;
constexpr int L_Q = 64;
constexpr int L_ML = L_Q + 1024;
constexpr int L_SC = L_ML + 512;
constexpr int L_PB = L_SC + 2048;
constexpr int L_V = L_PB + 11 * 1024;
constexpr int LA_END = L_V + 352 * 256;
constexpr int XN_STRIDE = 520;
constexpr int K3 = 736, K3_STRIDE = 744;
constexpr int L_XN = 64;
constexpr int L_A3 = L_XN + NB * XN_STRIDE * 2;
constexpr int L_RED = L_A3 + NB * K3_STRIDE * 2;
constexpr int L_WQ = L_RED + 2048;
constexpr int L_W1 = L_WQ + 65536;
constexpr int L_W3 = L_WQ;
constexpr int LB_END = L_W1 + 32768;
static_assert(L_W3 + K3 * 128 <= LB_END, "W3 overlay");
constexpr int LDS_BYTES = LA_END > LB_END ? LA_END : LB_END;
static_assert(LDS_BYTES > 80 * 1024 && LDS_BYTES <= 160 * 1024, "one workgroup per CU");
static_assert(L_WQ % 16 == 0 && L_A3 % 16 == 0 && L_V % 16 == 0 && L_PB % 16 == 0 && L_RED % 16 == 0, "align");

struct Params {
  const u16* x; const u16* kc; const u16* vc; const u16* wq; const u16* wo;
  const u16* w1; const u16* b1; const u16* w2; const u16* b2;
  u16* out; u64* ws;
};

__device__ __forceinline__ float bf2f(u32 h) { return __uint_as_float(h << 16); }
__device__ __forceinline__ u32 f2bf(float f) {
  __bf16 h = (__bf16)f;
  return (u32)__builtin_bit_cast(u16, h);
}
__device__ __forceinline__ float bfr(float f) { return bf2f(f2bf(f)); }

template <int CTRL> __device__ __forceinline__ float dpp(float x) {
  return __builtin_bit_cast(float, __builtin_amdgcn_mov_dpp(__builtin_bit_cast(int, x), CTRL, 0xf, 0xf, true));
}
template <int CTRL> __device__ __forceinline__ u32 dppu(u32 x) {
  return (u32)__builtin_amdgcn_mov_dpp((int)x, CTRL, 0xf, 0xf, true);
}
__device__ __forceinline__ float x16_max(float x) {
  auto s = __builtin_amdgcn_permlane16_swap(__float_as_uint(x), __float_as_uint(x), false, false);
  return fmaxf(__uint_as_float(s[0]), __uint_as_float(s[1]));
}
__device__ __forceinline__ float x32_max(float x) {
  auto t = __builtin_amdgcn_permlane32_swap(__float_as_uint(x), __float_as_uint(x), false, false);
  return fmaxf(__uint_as_float(t[0]), __uint_as_float(t[1]));
}
__device__ __forceinline__ float x16_sum(float x) {
  auto s = __builtin_amdgcn_permlane16_swap(__float_as_uint(x), __float_as_uint(x), false, false);
  return __uint_as_float(s[0]) + __uint_as_float(s[1]);
}
__device__ __forceinline__ float x32_sum(float x) {
  auto t = __builtin_amdgcn_permlane32_swap(__float_as_uint(x), __float_as_uint(x), false, false);
  return __uint_as_float(t[0]) + __uint_as_float(t[1]);
}

__device__ __forceinline__ void put_gran(u64* g, u32 v) {
  __hip_atomic_store((GAS u64*)g, ((u64)TAG << 32) | (u64)v, RLX_AGENT);
}
__device__ __forceinline__ u64 get_gran(const u64* g) {
  return __hip_atomic_load((GAS u64*)g, RLX_AGENT);
}
__device__ __forceinline__ bool tag_ok(u64 v) { return (u32)(v >> 32) == TAG; }

__device__ __forceinline__ void lds_wait() { asm volatile("s_waitcnt lgkmcnt(0)" ::: "memory"); }
__device__ __forceinline__ void vm_wait0() { asm volatile("s_waitcnt vmcnt(0)" ::: "memory"); }

typedef volatile LAS u32 lctr;
__device__ __forceinline__ void lds_add1(lctr* c) {
  __hip_atomic_fetch_add((LAS u32*)c, 1u, __ATOMIC_RELAXED, __HIP_MEMORY_SCOPE_WORKGROUP);
}
__device__ __forceinline__ void lds_arrive(lctr* c, int lane) {
  lds_wait();
  if (lane == 0) lds_add1(c);
  asm volatile("" ::: "memory");
}
__device__ __forceinline__ void lds_spin(lctr* c, u32 target) {
  for (u32 it = 0; it < SPIN_CAP; ++it) {
    if (*c >= target) break;
    __builtin_amdgcn_s_sleep(1);
  }
  asm volatile("" ::: "memory");
}

__device__ __forceinline__ float gelu_tanh(float v) {
  const float u = 0.7978845608028654f * (v + 0.044715f * v * v * v);
  return 0.5f * v * (1.f + tanhf(u));
}

__device__ __forceinline__ void dma16(const void* gsrc, unsigned char* lds_base) {
  __builtin_amdgcn_global_load_lds(gsrc, (LAS void*)lds_base, 16, 0, 2);
}
__device__ __forceinline__ bf16x8 bfrag_tr(const unsigned char* tile, int ks, int nt, int lane) {
  const int g = lane >> 4, q = (lane & 15) >> 2, pp = lane & 3;
  const unsigned char* a = tile + (32 * ks + 8 * g + q) * 128 + (16 * nt + 4 * pp) * 2;
  const s16x4 lo = __builtin_amdgcn_ds_read_tr16_b64_v4i16((LAS s16x4*)a);
  const s16x4 hi = __builtin_amdgcn_ds_read_tr16_b64_v4i16((LAS s16x4*)(a + 4 * 128));
  return bf16x8{lo[0], lo[1], lo[2], lo[3], hi[0], hi[1], hi[2], hi[3]};
}
__device__ __forceinline__ int vswz(int row) { return ((row & 3) << 2) | ((row >> 2) & 3); }

#define KV_WIN 5
#define Q_SLEEP 8
#define PG_SLEEP 4
#define STAMP(k) do { } while (0)

__global__ void __launch_bounds__(NTHR, 4) fused_decode(Params p) {
  __shared__ __attribute__((aligned(16))) unsigned char smem[LDS_BYTES];
  lctr* ctrl = (lctr*)(smem + L_CTRL);

  const int tid = threadIdx.x, lane = tid & 63;
  const int wave = __builtin_amdgcn_readfirstlane(tid >> 6);
  const int bid = blockIdx.x, xg = bid & 7, y = bid >> 3;
  const int g4 = lane >> 4, m16 = lane & 15;

  if (y < 24) {
    const int hkv = xg, bb = y / 3, sp = y - 3 * bb;
    const int nkw = sp == 2 ? 10 : 11;
    if (wave > 11 || (wave == 10 && nkw == 10)) return;
    if (wave == 11) {
      if (lane < 16) ctrl[lane] = 0u;
      lds_wait();
      __builtin_amdgcn_s_barrier();
      STAMP(6);
      const u64* g = p.ws + QG_OFF + (size_t)bb * 2048 + hkv * 256 + 4 * lane;
      u64 v[4];
      for (u32 it = 0; it < SPIN_CAP; ++it) {
#pragma unroll
        for (int k = 0; k < 4; ++k) v[k] = get_gran(g + k);
        if (__all(tag_ok(v[0]) && tag_ok(v[1]) && tag_ok(v[2]) && tag_ok(v[3]))) break;
        __builtin_amdgcn_s_sleep(Q_SLEEP);
      }
      *(u32x4*)(smem + L_Q + lane * 16) = u32x4{(u32)v[0], (u32)v[1], (u32)v[2], (u32)v[3]};
      lds_arrive(&ctrl[0], lane);
      STAMP(7);
      return;
    }
    if (wave == 0) STAMP(0);
    const size_t kvbase = ((size_t)(bb * HKV + hkv) * SK + KOFF + sp * 352 + wave * 32) * DH;
    const u32x4* kp = (const u32x4*)(p.kc + kvbase + (size_t)m16 * DH + g4 * 8);
    u32x4 kf[8];
#define KV_ISSUE(n) do { \
      if ((n) < 8) { kf[(n) & 7] = __builtin_nontemporal_load(kp + ((n) >> 2) * 16 * (DH / 8) + ((n) & 3) * 4); } \
      else { const int row_ = 32 * wave + 4 * ((n) - 8) + g4; \
             dma16(p.vc + kvbase + (size_t)(4 * ((n) - 8) + g4) * DH + ((m16 ^ vswz(row_)) * 8), smem + L_V + (32 * wave + 4 * ((n) - 8)) * 256); } \
      asm volatile("" ::: "memory"); } while (0)
#pragma unroll
    for (int n = 0; n < KV_WIN; ++n) KV_ISSUE(n);
    __builtin_amdgcn_s_barrier();
#pragma unroll
    for (int n = KV_WIN; n < 16; ++n) {
      asm volatile("s_waitcnt vmcnt(%0)" :: "i"(KV_WIN - 1) : "memory");
      KV_ISSUE(n);
    }
#undef KV_ISSUE
    if (wave == 0) STAMP(1);
    lds_spin(&ctrl[0], 1);
    if (wave == 0) STAMP(2);
    bf16x8 qf[4];
#pragma unroll
    for (int t = 0; t < 4; ++t) {
      bf16x8 z = {0, 0, 0, 0, 0, 0, 0, 0};
      if (m16 < 4) z = *(const bf16x8*)(smem + L_Q + (m16 * 128 + t * 32 + g4 * 8) * 2);
      qf[t] = z;
    }
    constexpr float SCALE = 0.08838834764831845f;
    float a[8];
#pragma unroll
    for (int T = 0; T < 2; ++T) {
      f32x4 s = {0.f, 0.f, 0.f, 0.f};
#pragma unroll
      for (int t = 0; t < 4; ++t) s = __builtin_amdgcn_mfma_f32_16x16x32_bf16(__builtin_bit_cast(bf16x8, kf[4 * T + t]), qf[t], s, 0, 0, 0);
#pragma unroll
      for (int i = 0; i < 4; ++i) a[4 * T + i] = bfr(bfr(s[i]) * SCALE);
    }
    float m = a[0];
#pragma unroll
    for (int i = 1; i < 8; ++i) m = fmaxf(m, a[i]);
    m = x32_max(x16_max(m));
    float l = 0.f;
    u32x4 pf;
#pragma unroll
    for (int i = 0; i < 4; ++i) {
      const u32 lo = f2bf(__builtin_amdgcn_exp2f((a[2 * i] - m) * LOG2E)), hi = f2bf(__builtin_amdgcn_exp2f((a[2 * i + 1] - m) * LOG2E));
      pf[i] = lo | (hi << 16);
      l += bf2f(lo) + bf2f(hi);
    }
    l = x32_sum(x16_sum(l));
    *(u32x4*)(smem + L_PB + (wave * 64 + lane) * 16) = pf;
    if (lane < 4) {
      float* ml = (float*)(smem + L_ML) + (wave * 4 + lane) * 2;
      ml[0] = m; ml[1] = l;
    }
    if (wave == 0) STAMP(3);
    vm_wait0();
    lds_arrive(&ctrl[1], lane);
    if (wave >= 8) return;
    lds_spin(&ctrl[1], (u32)nkw);
    if (wave == 0) STAMP(4);
    float Mx, Lx;
    {
      const int ws = lane >> 2, h = lane & 3;
      const bool live = ws < nkw;
      const float* ml = (const float*)(smem + L_ML) + ((live ? ws : 0) * 4 + h) * 2;
      const float mw = live ? ml[0] : -INFINITY, lw = live ? ml[1] : 0.f;
      float M = mw;
      M = fmaxf(M, dpp<0x124>(M)); M = fmaxf(M, dpp<0x128>(M)); M = x32_max(x16_max(M));
      const float sc = live ? __builtin_amdgcn_exp2f((mw - M) * LOG2E) : 0.f;
      float L = sc * lw;
      L += dpp<0x124>(L); L += dpp<0x128>(L); L = x32_sum(x16_sum(L));
      Mx = M; Lx = L;
      ((float*)(smem + L_SC))[wave * 64 + lane] = sc;
      lds_wait();
      __builtin_amdgcn_wave_barrier();
    }
    f32x4 o = {0.f, 0.f, 0.f, 0.f};
    {
      const int q = m16 >> 2, pp = lane & 3;
      const int ch = 2 * wave + (pp >> 1);
#pragma unroll
      for (int w = 0; w < 11; ++w) {
        if (w < 10 || nkw == 11) {
          const bf16x8 pa = *(const bf16x8*)(smem + L_PB + (w * 64 + lane) * 16);
          const int r0 = 32 * w + 4 * g4 + q, r1 = r0 + 16;
          const s16x4 lo = __builtin_amdgcn_ds_read_tr16_b64_v4i16((LAS s16x4*)(smem + L_V + r0 * 256 + ((ch ^ vswz(r0)) * 16) + 8 * (pp & 1)));
          const s16x4 hi = __builtin_amdgcn_ds_read_tr16_b64_v4i16((LAS s16x4*)(smem + L_V + r1 * 256 + ((ch ^ vswz(r1)) * 16) + 8 * (pp & 1)));
          const bf16x8 vb = {lo[0], lo[1], lo[2], lo[3], hi[0], hi[1], hi[2], hi[3]};
          const f32x4 z = {0.f, 0.f, 0.f, 0.f};
          const f32x4 d = __builtin_amdgcn_mfma_f32_16x16x32_bf16(pa, vb, z, 0, 0, 0);
          const f32x4 sc = *(const f32x4*)(smem + L_SC + (wave * 64 + w * 4) * 4);
#pragma unroll
          for (int i = 0; i < 4; ++i) o[i] = fmaf(sc[i], d[i], o[i]);
        }
      }
    }
    {
      u64* rec = p.ws + PG_OFF + (((size_t)(hkv * 4) * NB + bb) * NSP + sp) * PREC;
#pragma unroll
      for (int h = 0; h < 4; ++h) {
        const u32 mine = f2bf(o[h]);
        const u32 other = dppu<0xB1>(mine);
        if (lane < 16 && (lane & 1) == 0) put_gran(rec + (size_t)h * NB * NSP * PREC + 8 * wave + (lane >> 1), mine | (other << 16));
      }
      if (wave == 0 && lane < 4) {
        put_gran(rec + (size_t)lane * NB * NSP * PREC + 64, __float_as_uint(Mx));
        put_gran(rec + (size_t)lane * NB * NSP * PREC + 65, __float_as_uint(Lx));
      }
    }
    if (wave == 0) STAMP(5);
    return;
  }

  const int j = xg + 8 * (y - 24);
  if (wave == 0) STAMP(8);
  const bool has_ffn = j < 56;
  const int fcb = j % 28, fks = j / 28;
  const int rg = j >> 3, cg = j & 7;

  u32x4 xa = {0, 0, 0, 0};
  if (wave < 8) xa = *(const u32x4*)(p.x + wave * DM + lane * 8);
#pragma unroll
  for (int i = 0; i < 4; ++i) {
    const int r0 = 128 * i + 8 * wave;
    dma16(p.wq + (size_t)(r0 + (lane >> 3)) * 4096 + 64 * j + 8 * (lane & 7), smem + L_WQ + r0 * 128);
  }
  if (has_ffn) {
#pragma unroll
    for (int i = 0; i < 2; ++i) {
      const int r0 = 128 * i + 8 * wave;
      dma16(p.w1 + (size_t)(256 * fks + r0 + (lane >> 3)) * FF + 64 * fcb + 8 * (lane & 7), smem + L_W1 + r0 * 128);
    }
  }
  if (wave < 8) {
    float xv[8];
#pragma unroll
    for (int k = 0; k < 4; ++k) { xv[2 * k] = __uint_as_float(xa[k] << 16); xv[2 * k + 1] = __uint_as_float(xa[k] & 0xffff0000u); }
    float s = 0.f;
#pragma unroll
    for (int k = 0; k < 8; ++k) s += xv[k];
    s += dpp<0x121>(s); s += dpp<0x122>(s); s += dpp<0x124>(s); s += dpp<0x128>(s); s = x32_sum(x16_sum(s));
    const float mean = s * (1.f / 512.f);
    float v = 0.f;
#pragma unroll
    for (int k = 0; k < 8; ++k) { const float d = xv[k] - mean; v = fmaf(d, d, v); }
    v += dpp<0x121>(v); v += dpp<0x122>(v); v += dpp<0x124>(v); v += dpp<0x128>(v); v = x32_sum(x16_sum(v));
    const float mb = bfr(mean), vb = bfr(v * (1.f / 512.f));
    const float den = bfr(sqrtf(bfr(vb + 1e-5f)));
    u32x4 o;
#pragma unroll
    for (int k = 0; k < 4; ++k) {
      const float a = bfr(xv[2 * k] - mb) / den, b = bfr(xv[2 * k + 1] - mb) / den;
      o[k] = f2bf(a) | (f2bf(b) << 16);
    }
    *(u32x4*)(smem + L_XN + (wave * XN_STRIDE + lane * 8) * 2) = o;
  }
  vm_wait0();
  lds_wait();
  __builtin_amdgcn_s_barrier();
  if (wave == 0) STAMP(9);
  if (wave < 4) {
    f32x4 d = {0.f, 0.f, 0.f, 0.f};
#pragma unroll
    for (int ks = 0; ks < 16; ++ks) {
      bf16x8 a = {0, 0, 0, 0, 0, 0, 0, 0};
      if (m16 < 8) a = *(const bf16x8*)(smem + L_XN + (m16 * XN_STRIDE + ks * 32 + g4 * 8) * 2);
      d = __builtin_amdgcn_mfma_f32_16x16x32_bf16(a, bfrag_tr(smem + L_WQ, ks, wave, lane), d, 0, 0, 0);
    }
#pragma unroll
    for (int i = 0; i < 4; ++i) {
      const u32 mine = f2bf(d[i]);
      const u32 other = dppu<0xB1>(mine);
      if (lane < 32 && (lane & 1) == 0)
        put_gran(p.ws + QG_OFF + (size_t)(4 * g4 + i) * 2048 + 32 * j + 8 * wave + (m16 >> 1), mine | (other << 16));
    }
  } else if (wave < 8 && has_ffn) {
    f32x4 e = {0.f, 0.f, 0.f, 0.f};
#pragma unroll
    for (int ks = 0; ks < 8; ++ks) {
      bf16x8 a = {0, 0, 0, 0, 0, 0, 0, 0};
      if (m16 < 8) a = *(const bf16x8*)(smem + L_XN + (m16 * XN_STRIDE + 256 * fks + ks * 32 + g4 * 8) * 2);
      e = __builtin_amdgcn_mfma_f32_16x16x32_bf16(a, bfrag_tr(smem + L_W1, ks, wave - 4, lane), e, 0, 0, 0);
    }
    if (lane < 32) {
#pragma unroll
      for (int i = 0; i < 4; ++i)
        put_gran(p.ws + HG_OFF + ((size_t)(4 * g4 + i) * 2 + fks) * FF + 64 * fcb + 16 * (wave - 4) + m16, __float_as_uint(e[i]));
    }
  }
  lds_wait();
  __builtin_amdgcn_s_barrier();
  if (wave == 0) STAMP(10);
#pragma unroll
  for (int i = 0; i < 6; ++i) {
    const int r0 = 128 * i + 8 * wave;
    if (r0 < K3) {
      const int r = r0 + (lane >> 3);
      const u16* src = r0 < 512 ? (p.wo + (size_t)(512 * rg + r) * DM) : (p.w2 + (size_t)(224 * rg + r - 512) * DM);
      dma16(src + 64 * cg + 8 * (lane & 7), smem + L_W3 + r0 * 128);
    }
  }
  const int ocol = 64 * cg + 8 * rg;
  float xres = 0.f;
  if (tid < 64) xres = bf2f(p.x[(tid >> 3) * DM + ocol + (tid & 7)]) + bf2f(p.b2[ocol + (tid & 7)]);
  if (tid < 896) {
    const int b = tid / 112, jj = tid - 112 * b;
    const u64* g = p.ws + HG_OFF + (size_t)b * 2 * FF + 224 * rg + 2 * jj;
    const u32 bias = *(const u32*)(p.b1 + 224 * rg + 2 * jj);
    u64 v[4];
    for (u32 it = 0; it < SPIN_CAP; ++it) {
      v[0] = get_gran(g); v[1] = get_gran(g + 1); v[2] = get_gran(g + FF); v[3] = get_gran(g + FF + 1);
      if (tag_ok(v[0]) && tag_ok(v[1]) && tag_ok(v[2]) && tag_ok(v[3])) break;
      __builtin_amdgcn_s_sleep(4);
    }
    float h0 = __uint_as_float((u32)v[0]) + __uint_as_float((u32)v[2]);
    float h1 = __uint_as_float((u32)v[1]) + __uint_as_float((u32)v[3]);
    h0 = gelu_tanh(bfr(bfr(h0) + bf2f(bias & 0xffffu)));
    h1 = gelu_tanh(bfr(bfr(h1) + bf2f(bias >> 16)));
    ((u32*)(smem + L_A3))[b * (K3_STRIDE / 2) + 256 + jj] = f2bf(h0) | (f2bf(h1) << 16);
  }
  if (wave == 0) STAMP(11);
  {
    const int hh = tid >> 8, b = (tid >> 5) & 7, jj = tid & 31;
    const u64* base = p.ws + PG_OFF + ((size_t)(4 * rg + hh) * NB + b) * NSP * PREC;
    u64 ov[2 * NSP], mv[NSP], lv[NSP];
    for (u32 it = 0; it < SPIN_CAP; ++it) {
      bool ok = true;
#pragma unroll
      for (int s = 0; s < NSP; ++s) {
        ov[2 * s] = get_gran(base + s * PREC + 2 * jj);
        ov[2 * s + 1] = get_gran(base + s * PREC + 2 * jj + 1);
        mv[s] = get_gran(base + s * PREC + 64);
        lv[s] = get_gran(base + s * PREC + 65);
      }
#pragma unroll
      for (int s = 0; s < NSP; ++s) ok = ok && tag_ok(ov[2 * s]) && tag_ok(ov[2 * s + 1]) && tag_ok(mv[s]) && tag_ok(lv[s]);
      if (__all(ok)) break;
      __builtin_amdgcn_s_sleep(PG_SLEEP);
    }
    float M = -INFINITY;
#pragma unroll
    for (int s = 0; s < NSP; ++s) M = fmaxf(M, __uint_as_float((u32)mv[s]));
    float L = 0.f, o[4] = {0.f, 0.f, 0.f, 0.f};
#pragma unroll
    for (int s = 0; s < NSP; ++s) {
      const float wt = __builtin_amdgcn_exp2f((__uint_as_float((u32)mv[s]) - M) * LOG2E);
      L = fmaf(wt, __uint_as_float((u32)lv[s]), L);
      o[0] = fmaf(wt, bf2f((u32)ov[2 * s] & 0xffffu), o[0]);
      o[1] = fmaf(wt, bf2f(((u32)ov[2 * s]) >> 16), o[1]);
      o[2] = fmaf(wt, bf2f((u32)ov[2 * s + 1] & 0xffffu), o[2]);
      o[3] = fmaf(wt, bf2f(((u32)ov[2 * s + 1]) >> 16), o[3]);
    }
    const float inv = 1.f / L;
    u32* a3 = (u32*)(smem + L_A3) + b * (K3_STRIDE / 2) + 64 * hh + 2 * jj;
    a3[0] = f2bf(o[0] * inv) | (f2bf(o[1] * inv) << 16);
    a3[1] = f2bf(o[2] * inv) | (f2bf(o[3] * inv) << 16);
  }
  if (wave == 0) STAMP(12);
  vm_wait0();
  lds_wait();
  __builtin_amdgcn_s_barrier();
  if (wave == 0) STAMP(13);
  if (wave < 4) {
    f32x4 d = {0.f, 0.f, 0.f, 0.f};
#pragma unroll
    for (int ks = 0; ks < 23; ++ks) {
      bf16x8 a = {0, 0, 0, 0, 0, 0, 0, 0};
      if (m16 < 8) a = *(const bf16x8*)(smem + L_A3 + (m16 * K3_STRIDE + ks * 32 + g4 * 8) * 2);
      d = __builtin_amdgcn_mfma_f32_16x16x32_bf16(a, bfrag_tr(smem + L_W3, ks, wave, lane), d, 0, 0, 0);
    }
    if (lane < 32) {
      const int col = 16 * wave + m16;
      u64* og = p.ws + OG_OFF + ((((size_t)cg * 8 + (col >> 3)) * 8 + rg) * 8) * 8 + (col & 7);
#pragma unroll
      for (int i = 0; i < 4; ++i) put_gran(og + (4 * g4 + i) * 8, __float_as_uint(d[i]));
    }
  }
  if (wave == 0) STAMP(14);
  if (wave < 8) {
    const u64* base = p.ws + OG_OFF + (((size_t)cg * 8 + rg) * 8) * 64 + tid;
    u64 a = 0;
    for (u32 it = 0; it < SPIN_CAP; ++it) {
      a = get_gran(base);
      if (__all(tag_ok(a))) break;
      __builtin_amdgcn_s_sleep(1);
    }
    ((float*)(smem + L_RED))[tid] = __uint_as_float((u32)a);
  }
  if (wave == 0) STAMP(15);
  lds_wait();
  __builtin_amdgcn_s_barrier();
  if (tid < 64) {
    const float* red = (const float*)(smem + L_RED);
    float r = xres;
#pragma unroll
    for (int w = 0; w < 8; ++w) r += red[w * 64 + tid];
    p.out[(tid >> 3) * DM + ocol + (tid & 7)] = (u16)f2bf(r);
  }
  if (wave == 0) STAMP(16);
}

}

extern "C" void kernel_launch(void* const* d_in, const int* in_sizes, int n_in,
                              void* d_out, int out_size, void* d_ws, size_t ws_size,
                              hipStream_t stream) {
  Params p;
  p.x = (const u16*)d_in[0];
  p.kc = (const u16*)d_in[1];
  p.vc = (const u16*)d_in[2];
  p.wq = (const u16*)d_in[3];
  p.wo = (const u16*)d_in[4];
  p.w1 = (const u16*)d_in[6];
  p.b1 = (const u16*)d_in[7];
  p.w2 = (const u16*)d_in[8];
  p.b2 = (const u16*)d_in[9];
  p.out = (u16*)d_out;
  p.ws = (u64*)d_ws;
  (void)hipMemsetAsync(d_ws, 0, WS_BYTES, stream);
  hipLaunchKernelGGL(fused_decode, dim3(NWG), dim3(NTHR), 0, stream, p);
}
```

```cpp
#include <hip/hip_runtime.h>
#include <hip/hip_bf16.h>
#include <stdint.h>

typedef unsigned int u32;
typedef unsigned long long u64;
typedef unsigned short u16;
typedef u32 u32x4 __attribute__((ext_vector_type(4)));
typedef float f32x4 __attribute__((ext_vector_type(4)));
typedef short bf16x8 __attribute__((ext_vector_type(8)));
typedef short s16x4 __attribute__((ext_vector_type(4)));

#define GAS __attribute__((address_space(1)))
#define LAS __attribute__((address_space(3)))
#define RLX_AGENT __ATOMIC_RELAXED, __HIP_MEMORY_SCOPE_AGENT

namespace {

constexpr int NB = 8, DM = 512, HQ = 32, HKV = 8, SK = 4096, DH = 128, WINDOW = 1024, FF = 1792;
constexpr int KOFF = SK - WINDOW;
constexpr int NWG = 256, NTHR = 1024;
constexpr u32 TAG = 0x3C5A0001u;
constexpr u32 SPIN_CAP = 1u << 22;
constexpr float LOG2E = 1.4426950408889634f;

constexpr int NSP = 3;
constexpr int PREC = 66;
constexpr size_t QG_OFF = 0, QG_N = (size_t)NB * 2048;
constexpr size_t HG_OFF = QG_OFF + QG_N, HG_N = (size_t)NB * 2 * FF;
constexpr size_t PG_OFF = HG_OFF + HG_N, PG_N = (size_t)HQ * NB * NSP * PREC;
constexpr size_t OG_OFF = PG_OFF + PG_N, OG_N = (size_t)8 * 8 * 8 * 8 * 8;
constexpr size_t WS_GRAN = OG_OFF + OG_N;
constexpr size_t WS_BYTES = WS_GRAN * 8;
static_assert(WS_BYTES % 16 == 0, "memset block must be a multiple of 16 bytes");

constexpr int L_CTRL = 0;
constexpr int L_Q = 64;
constexpr int L_ML = L_Q + 1024;
constexpr int L_SC = L_ML + 512;
constexpr int L_PB = L_SC + 2048;
constexpr int L_V = L_PB + 11 * 1024;
constexpr int LA_END = L_V + 352 * 256;
constexpr int XN_STRIDE = 520;
constexpr int K3 = 736, K3_STRIDE = 744;
constexpr int L_XN = 64;
constexpr int L_A3 = L_XN + NB * XN_STRIDE * 2;
constexpr int L_RED = L_A3 + NB * K3_STRIDE * 2;
constexpr int L_WQ = L_RED + 2048;
constexpr int L_W1 = L_WQ + 65536;
constexpr int L_W3 = L_WQ;
constexpr int LB_END = L_W1 + 32768;
static_assert(L_W3 + K3 * 128 <= LB_END, "W3 overlay");
constexpr int LDS_BYTES = LA_END > LB_END ? LA_END : LB_END;
static_assert(LDS_BYTES > 80 * 1024 && LDS_BYTES <= 160 * 1024, "one workgroup per CU");
static_assert(L_WQ % 16 == 0 && L_A3 % 16 == 0 && L_V % 16 == 0 && L_PB % 16 == 0 && L_RED % 16 == 0, "align");

struct Params {
  const u16* x; const u16* kc; const u16* vc; const u16* wq; const u16* wo;
  const u16* w1; const u16* b1; const u16* w2; const u16* b2;
  u16* out; u64* ws;
};

__device__ __forceinline__ float bf2f(u32 h) { return __uint_as_float(h << 16); }
__device__ __forceinline__ u32 f2bf(float f) {
  __bf16 h = (__bf16)f;
  return (u32)__builtin_bit_cast(u16, h);
}
__device__ __forceinline__ float bfr(float f) { return bf2f(f2bf(f)); }

template <int CTRL> __device__ __forceinline__ float dpp(float x) {
  return __builtin_bit_cast(float, __builtin_amdgcn_mov_dpp(__builtin_bit_cast(int, x), CTRL, 0xf, 0xf, true));
}
template <int CTRL> __device__ __forceinline__ u32 dppu(u32 x) {
  return (u32)__builtin_amdgcn_mov_dpp((int)x, CTRL, 0xf, 0xf, true);
}
__device__ __forceinline__ float x16_max(float x) {
  auto s = __builtin_amdgcn_permlane16_swap(__float_as_uint(x), __float_as_uint(x), false, false);
  return fmaxf(__uint_as_float(s[0]), __uint_as_float(s[1]));
}
__device__ __forceinline__ float x32_max(float x) {
  auto t = __builtin_amdgcn_permlane32_swap(__float_as_uint(x), __float_as_uint(x), false, false);
  return fmaxf(__uint_as_float(t[0]), __uint_as_float(t[1]));
}
__device__ __forceinline__ float x16_sum(float x) {
  auto s = __builtin_amdgcn_permlane16_swap(__float_as_uint(x), __float_as_uint(x), false, false);
  return __uint_as_float(s[0]) + __uint_as_float(s[1]);
}
__device__ __forceinline__ float x32_sum(float x) {
  auto t = __builtin_amdgcn_permlane32_swap(__float_as_uint(x), __float_as_uint(x), false, false);
  return __uint_as_float(t[0]) + __uint_as_float(t[1]);
}

__device__ __forceinline__ void put_gran(u64* g, u32 v) {
  __hip_atomic_store((GAS u64*)g, ((u64)TAG << 32) | (u64)v, RLX_AGENT);
}
__device__ __forceinline__ u64 get_gran(const u64* g) {
  return __hip_atomic_load((GAS u64*)g, RLX_AGENT);
}
__device__ __forceinline__ bool tag_ok(u64 v) { return (u32)(v >> 32) == TAG; }
__device__ __forceinline__ u32x4 get_gran2(__amdgpu_buffer_rsrc_t rs, size_t gidx) {
  return __builtin_amdgcn_raw_buffer_load_b128(rs, (int)(gidx * 8), 0, 16);
}

__device__ __forceinline__ void lds_wait() { asm volatile("s_waitcnt lgkmcnt(0)" ::: "memory"); }
__device__ __forceinline__ void vm_wait0() { asm volatile("s_waitcnt vmcnt(0)" ::: "memory"); }

typedef volatile LAS u32 lctr;
__device__ __forceinline__ void lds_add1(lctr* c) {
  __hip_atomic_fetch_add((LAS u32*)c, 1u, __ATOMIC_RELAXED, __HIP_MEMORY_SCOPE_WORKGROUP);
}
__device__ __forceinline__ void lds_arrive(lctr* c, int lane) {
  lds_wait();
  if (lane == 0) lds_add1(c);
  asm volatile("" ::: "memory");
}
__device__ __forceinline__ void lds_spin(lctr* c, u32 target) {
  for (u32 it = 0; it < SPIN_CAP; ++it) {
    if (*c >= target) break;
    __builtin_amdgcn_s_sleep(1);
  }
  asm volatile("" ::: "memory");
}

__device__ __forceinline__ float gelu_tanh(float v) {
  const float u = 0.7978845608028654f * (v + 0.044715f * v * v * v);
  return 0.5f * v * (1.f + tanhf(u));
}

__device__ __forceinline__ void dma16(const void* gsrc, unsigned char* lds_base) {
  __builtin_amdgcn_global_load_lds(gsrc, (LAS void*)lds_base, 16, 0, 2);
}
__device__ __forceinline__ bf16x8 bfrag_tr(const unsigned char* tile, int ks, int nt, int lane) {
  const int g = lane >> 4, q = (lane & 15) >> 2, pp = lane & 3;
  const unsigned char* a = tile + (32 * ks + 8 * g + q) * 128 + (16 * nt + 4 * pp) * 2;
  const s16x4 lo = __builtin_amdgcn_ds_read_tr16_b64_v4i16((LAS s16x4*)a);
  const s16x4 hi = __builtin_amdgcn_ds_read_tr16_b64_v4i16((LAS s16x4*)(a + 4 * 128));
  return bf16x8{lo[0], lo[1], lo[2], lo[3], hi[0], hi[1], hi[2], hi[3]};
}
__device__ __forceinline__ int vswz(int row) { return ((row & 3) << 2) | ((row >> 2) & 3); }

#define KV_WIN 5
#define Q_SLEEP 8
#define PG_SLEEP 4
#define STAMP(k) do { } while (0)

__global__ void __launch_bounds__(NTHR, 4) fused_decode(Params p) {
  __shared__ __attribute__((aligned(16))) unsigned char smem[LDS_BYTES];
  lctr* ctrl = (lctr*)(smem + L_CTRL);

  const int tid = threadIdx.x, lane = tid & 63;
  const int wave = __builtin_amdgcn_readfirstlane(tid >> 6);
  const int bid = blockIdx.x, xg = bid & 7, y = bid >> 3;
  const int g4 = lane >> 4, m16 = lane & 15;

  if (y < 24) {
    const int hkv = xg, bb = y / 3, sp = y - 3 * bb;
    const int nkw = sp == 2 ? 10 : 11;
    if (wave > 11 || (wave == 10 && nkw == 10)) return;
    if (wave == 11) {
      if (lane < 16) ctrl[lane] = 0u;
      lds_wait();
      __builtin_amdgcn_s_barrier();
      STAMP(6);
      const u64* g = p.ws + QG_OFF + (size_t)bb * 2048 + hkv * 256 + 4 * lane;
      u64 v[4];
      for (u32 it = 0; it < SPIN_CAP; ++it) {
#pragma unroll
        for (int k = 0; k < 4; ++k) v[k] = get_gran(g + k);
        if (__all(tag_ok(v[0]) && tag_ok(v[1]) && tag_ok(v[2]) && tag_ok(v[3]))) break;
        __builtin_amdgcn_s_sleep(Q_SLEEP);
      }
      *(u32x4*)(smem + L_Q + lane * 16) = u32x4{(u32)v[0], (u32)v[1], (u32)v[2], (u32)v[3]};
      lds_arrive(&ctrl[0], lane);
      STAMP(7);
      return;
    }
    if (wave == 0) STAMP(0);
    const size_t kvbase = ((size_t)(bb * HKV + hkv) * SK + KOFF + sp * 352 + wave * 32) * DH;
    const u32x4* kp = (const u32x4*)(p.kc + kvbase + (size_t)m16 * DH + g4 * 8);
    u32x4 kf[8];
#define KV_ISSUE(n) do { \
      if ((n) < 8) { kf[(n) & 7] = __builtin_nontemporal_load(kp + ((n) >> 2) * 16 * (DH / 8) + ((n) & 3) * 4); } \
      else { const int row_ = 32 * wave + 4 * ((n) - 8) + g4; \
             dma16(p.vc + kvbase + (size_t)(4 * ((n) - 8) + g4) * DH + ((m16 ^ vswz(row_)) * 8), smem + L_V + (32 * wave + 4 * ((n) - 8)) * 256); } \
      asm volatile("" ::: "memory"); } while (0)
#pragma unroll
    for (int n = 0; n < KV_WIN; ++n) KV_ISSUE(n);
    __builtin_amdgcn_s_barrier();
#pragma unroll
    for (int n = KV_WIN; n < 16; ++n) {
      asm volatile("s_waitcnt vmcnt(%0)" :: "i"(KV_WIN - 1) : "memory");
      KV_ISSUE(n);
    }
#undef KV_ISSUE
    if (wave == 0) STAMP(1);
    lds_spin(&ctrl[0], 1);
    if (wave == 0) STAMP(2);
    bf16x8 qf[4];
#pragma unroll
    for (int t = 0; t < 4; ++t) {
      bf16x8 z = {0, 0, 0, 0, 0, 0, 0, 0};
      if (m16 < 4) z = *(const bf16x8*)(smem + L_Q + (m16 * 128 + t * 32 + g4 * 8) * 2);
      qf[t] = z;
    }
    constexpr float SCALE = 0.08838834764831845f;
    float a[8];
#pragma unroll
    for (int T = 0; T < 2; ++T) {
      f32x4 s = {0.f, 0.f, 0.f, 0.f};
#pragma unroll
      for (int t = 0; t < 4; ++t) s = __builtin_amdgcn_mfma_f32_16x16x32_bf16(__builtin_bit_cast(bf16x8, kf[4 * T + t]), qf[t], s, 0, 0, 0);
#pragma unroll
      for (int i = 0; i < 4; ++i) a[4 * T + i] = bfr(bfr(s[i]) * SCALE);
    }
    float m = a[0];
#pragma unroll
    for (int i = 1; i < 8; ++i) m = fmaxf(m, a[i]);
    m = x32_max(x16_max(m));
    float l = 0.f;
    u32x4 pf;
#pragma unroll
    for (int i = 0; i < 4; ++i) {
      const u32 lo = f2bf(__builtin_amdgcn_exp2f((a[2 * i] - m) * LOG2E)), hi = f2bf(__builtin_amdgcn_exp2f((a[2 * i + 1] - m) * LOG2E));
      pf[i] = lo | (hi << 16);
      l += bf2f(lo) + bf2f(hi);
    }
    l = x32_sum(x16_sum(l));
    *(u32x4*)(smem + L_PB + (wave * 64 + lane) * 16) = pf;
    if (lane < 4) {
      float* ml = (float*)(smem + L_ML) + (wave * 4 + lane) * 2;
      ml[0] = m; ml[1] = l;
    }
    if (wave == 0) STAMP(3);
    vm_wait0();
    lds_arrive(&ctrl[1], lane);
    if (wave >= 8) return;
    lds_spin(&ctrl[1], (u32)nkw);
    if (wave == 0) STAMP(4);
    float Mx, Lx;
    {
      const int ws = lane >> 2, h = lane & 3;
      const bool live = ws < nkw;
      const float* ml = (const float*)(smem + L_ML) + ((live ? ws : 0) * 4 + h) * 2;
      const float mw = live ? ml[0] : -INFINITY, lw = live ? ml[1] : 0.f;
      float M = mw;
      M = fmaxf(M, dpp<0x124>(M)); M = fmaxf(M, dpp<0x128>(M)); M = x32_max(x16_max(M));
      const float sc = live ? __builtin_amdgcn_exp2f((mw - M) * LOG2E) : 0.f;
      float L = sc * lw;
      L += dpp<0x124>(L); L += dpp<0x128>(L); L = x32_sum(x16_sum(L));
      Mx = M; Lx = L;
      ((float*)(smem + L_SC))[wave * 64 + lane] = sc;
      lds_wait();
      __builtin_amdgcn_wave_barrier();
    }
    f32x4 o = {0.f, 0.f, 0.f, 0.f};
    {
      const int q = m16 >> 2, pp = lane & 3;
      const int ch = 2 * wave + (pp >> 1);
#pragma unroll
      for (int w = 0; w < 11; ++w) {
        if (w < 10 || nkw == 11) {
          const bf16x8 pa = *(const bf16x8*)(smem + L_PB + (w * 64 + lane) * 16);
          const int r0 = 32 * w + 4 * g4 + q, r1 = r0 + 16;
          const s16x4 lo = __builtin_amdgcn_ds_read_tr16_b64_v4i16((LAS s16x4*)(smem + L_V + r0 * 256 + ((ch ^ vswz(r0)) * 16) + 8 * (pp & 1)));
          const s16x4 hi = __builtin_amdgcn_ds_read_tr16_b64_v4i16((LAS s16x4*)(smem + L_V + r1 * 256 + ((ch ^ vswz(r1)) * 16) + 8 * (pp & 1)));
          const bf16x8 vb = {lo[0], lo[1], lo[2], lo[3], hi[0], hi[1], hi[2], hi[3]};
          const f32x4 z = {0.f, 0.f, 0.f, 0.f};
          const f32x4 d = __builtin_amdgcn_mfma_f32_16x16x32_bf16(pa, vb, z, 0, 0, 0);
          const f32x4 sc = *(const f32x4*)(smem + L_SC + (wave * 64 + w * 4) * 4);
#pragma unroll
          for (int i = 0; i < 4; ++i) o[i] = fmaf(sc[i], d[i], o[i]);
        }
      }
    }
    {
      u64* rec = p.ws + PG_OFF + (((size_t)(hkv * 4) * NB + bb) * NSP + sp) * PREC;
#pragma unroll
      for (int h = 0; h < 4; ++h) {
        const u32 mine = f2bf(o[h]);
        const u32 other = dppu<0xB1>(mine);
        if (lane < 16 && (lane & 1) == 0) put_gran(rec + (size_t)h * NB * NSP * PREC + 8 * wave + (lane >> 1), mine | (other << 16));
      }
      if (wave == 0 && lane < 4) {
        put_gran(rec + (size_t)lane * NB * NSP * PREC + 64, __float_as_uint(Mx));
        put_gran(rec + (size_t)lane * NB * NSP * PREC + 65, __float_as_uint(Lx));
      }
    }
    if (wave == 0) STAMP(5);
    return;
  }

  const int j = xg + 8 * (y - 24);
  if (wave == 0) STAMP(8);
  if (tid < 16) ctrl[tid] = 0u;
  const bool has_ffn = j < 56;
  const int fcb = j % 28, fks = j / 28;
  const int rg = j >> 3, cg = j & 7;

  u32x4 xa = {0, 0, 0, 0};
  if (wave < 8) xa = *(const u32x4*)(p.x + wave * DM + lane * 8);
#pragma unroll
  for (int i = 0; i < 4; ++i) {
    const int r0 = 128 * i + 8 * wave;
    dma16(p.wq + (size_t)(r0 + (lane >> 3)) * 4096 + 64 * j + 8 * (lane & 7), smem + L_WQ + r0 * 128);
  }
  if (has_ffn) {
#pragma unroll
    for (int i = 0; i < 2; ++i) {
      const int r0 = 128 * i + 8 * wave;
      dma16(p.w1 + (size_t)(256 * fks + r0 + (lane >> 3)) * FF + 64 * fcb + 8 * (lane & 7), smem + L_W1 + r0 * 128);
    }
  }
  if (wave < 8) {
    float xv[8];
#pragma unroll
    for (int k = 0; k < 4; ++k) { xv[2 * k] = __uint_as_float(xa[k] << 16); xv[2 * k + 1] = __uint_as_float(xa[k] & 0xffff0000u); }
    float s = 0.f;
#pragma unroll
    for (int k = 0; k < 8; ++k) s += xv[k];
    s += dpp<0x121>(s); s += dpp<0x122>(s); s += dpp<0x124>(s); s += dpp<0x128>(s); s = x32_sum(x16_sum(s));
    const float mean = s * (1.f / 512.f);
    float v = 0.f;
#pragma unroll
    for (int k = 0; k < 8; ++k) { const float d = xv[k] - mean; v = fmaf(d, d, v); }
    v += dpp<0x121>(v); v += dpp<0x122>(v); v += dpp<0x124>(v); v += dpp<0x128>(v); v = x32_sum(x16_sum(v));
    const float mb = bfr(mean), vb = bfr(v * (1.f / 512.f));
    const float den = bfr(sqrtf(bfr(vb + 1e-5f)));
    u32x4 o;
#pragma unroll
    for (int k = 0; k < 4; ++k) {
      const float a = bfr(xv[2 * k] - mb) / den, b = bfr(xv[2 * k + 1] - mb) / den;
      o[k] = f2bf(a) | (f2bf(b) << 16);
    }
    *(u32x4*)(smem + L_XN + (wave * XN_STRIDE + lane * 8) * 2) = o;
  }
  vm_wait0();
  lds_wait();
  __builtin_amdgcn_s_barrier();
  if (wave == 0) STAMP(9);
  if (wave < 4) {
    f32x4 d = {0.f, 0.f, 0.f, 0.f};
#pragma unroll
    for (int ks = 0; ks < 16; ++ks) {
      bf16x8 a = {0, 0, 0, 0, 0, 0, 0, 0};
      if (m16 < 8) a = *(const bf16x8*)(smem + L_XN + (m16 * XN_STRIDE + ks * 32 + g4 * 8) * 2);
      d = __builtin_amdgcn_mfma_f32_16x16x32_bf16(a, bfrag_tr(smem + L_WQ, ks, wave, lane), d, 0, 0, 0);
    }
#pragma unroll
    for (int i = 0; i < 4; ++i) {
      const u32 mine = f2bf(d[i]);
      const u32 other = dppu<0xB1>(mine);
      if (lane < 32 && (lane & 1) == 0)
        put_gran(p.ws + QG_OFF + (size_t)(4 * g4 + i) * 2048 + 32 * j + 8 * wave + (m16 >> 1), mine | (other << 16));
    }
  } else if (wave < 8 && has_ffn) {
    f32x4 e = {0.f, 0.f, 0.f, 0.f};
#pragma unroll
    for (int ks = 0; ks < 8; ++ks) {
      bf16x8 a = {0, 0, 0, 0, 0, 0, 0, 0};
      if (m16 < 8) a = *(const bf16x8*)(smem + L_XN + (m16 * XN_STRIDE + 256 * fks + ks * 32 + g4 * 8) * 2);
      e = __builtin_amdgcn_mfma_f32_16x16x32_bf16(a, bfrag_tr(smem + L_W1, ks, wave - 4, lane), e, 0, 0, 0);
    }
    if (lane < 32) {
#pragma unroll
      for (int i = 0; i < 4; ++i)
        put_gran(p.ws + HG_OFF + ((size_t)(4 * g4 + i) * 2 + fks) * FF + 64 * fcb + 16 * (wave - 4) + m16, __float_as_uint(e[i]));
    }
  }
  lds_wait();
  __builtin_amdgcn_s_barrier();
  if (wave == 0) STAMP(10);
#pragma unroll
  for (int i = 0; i < 6; ++i) {
    const int r0 = 128 * i + 8 * wave;
    if (r0 < K3) {
      const int r = r0 + (lane >> 3);
      const u16* src = r0 < 512 ? (p.wo + (size_t)(512 * rg + r) * DM) : (p.w2 + (size_t)(224 * rg + r - 512) * DM);
      dma16(src + 64 * cg + 8 * (lane & 7), smem + L_W3 + r0 * 128);
    }
  }
  const int ocol = 64 * cg + 8 * rg;
  float xres = 0.f;
  if (tid < 64) xres = bf2f(p.x[(tid >> 3) * DM + ocol + (tid & 7)]) + bf2f(p.b2[ocol + (tid & 7)]);
  if (tid < 896) {
    const int b = tid / 112, jj = tid - 112 * b;
    const u64* g = p.ws + HG_OFF + (size_t)b * 2 * FF + 224 * rg + 2 * jj;
    const u32 bias = *(const u32*)(p.b1 + 224 * rg + 2 * jj);
    u64 v[4];
    for (u32 it = 0; it < SPIN_CAP; ++it) {
      v[0] = get_gran(g); v[1] = get_gran(g + 1); v[2] = get_gran(g + FF); v[3] = get_gran(g + FF + 1);
      if (tag_ok(v[0]) && tag_ok(v[1]) && tag_ok(v[2]) && tag_ok(v[3])) break;
      __builtin_amdgcn_s_sleep(4);
    }
    float h0 = __uint_as_float((u32)v[0]) + __uint_as_float((u32)v[2]);
    float h1 = __uint_as_float((u32)v[1]) + __uint_as_float((u32)v[3]);
    h0 = gelu_tanh(bfr(bfr(h0) + bf2f(bias & 0xffffu)));
    h1 = gelu_tanh(bfr(bfr(h1) + bf2f(bias >> 16)));
    ((u32*)(smem + L_A3))[b * (K3_STRIDE / 2) + 256 + jj] = f2bf(h0) | (f2bf(h1) << 16);
  }
  if (wave == 0) STAMP(11);
  if (wave == 15) {
    const u64* base = p.ws + PG_OFF + (size_t)(4 * rg) * NB * NSP * PREC + 65;
    for (u32 it = 0; it < SPIN_CAP; ++it) {
      const u64 a = get_gran(base + (size_t)lane * PREC);
      u64 b = 0;
      if (lane < 32) b = get_gran(base + (size_t)(64 + lane) * PREC);
      if (__all(tag_ok(a) && (lane >= 32 || tag_ok(b)))) break;
      __builtin_amdgcn_s_sleep(PG_SLEEP);
    }
    lds_arrive(&ctrl[4], lane);
  } else {
    lds_spin(&ctrl[4], 1);
  }
  {
    const int hh = tid >> 8, b = (tid >> 5) & 7, jj = tid & 31;
    const size_t base = PG_OFF + ((size_t)(4 * rg + hh) * NB + b) * NSP * PREC;
    const __amdgpu_buffer_rsrc_t rs = __builtin_amdgcn_make_buffer_rsrc((void*)p.ws, 0, (int)WS_BYTES, 0x00020000);
    u32x4 ov[NSP], ml[NSP];
    for (u32 it = 0; it < SPIN_CAP; ++it) {
      bool ok = true;
#pragma unroll
      for (int s = 0; s < NSP; ++s) {
        ov[s] = get_gran2(rs, base + s * PREC + 2 * jj);
        ml[s] = get_gran2(rs, base + s * PREC + 64);
      }
#pragma unroll
      for (int s = 0; s < NSP; ++s) ok = ok && ov[s][1] == TAG && ov[s][3] == TAG && ml[s][1] == TAG && ml[s][3] == TAG;
      if (__all(ok)) break;
      __builtin_amdgcn_s_sleep(1);
    }
    float M = -INFINITY;
#pragma unroll
    for (int s = 0; s < NSP; ++s) M = fmaxf(M, __uint_as_float(ml[s][0]));
    float L = 0.f, o[4] = {0.f, 0.f, 0.f, 0.f};
#pragma unroll
    for (int s = 0; s < NSP; ++s) {
      const float wt = __builtin_amdgcn_exp2f((__uint_as_float(ml[s][0]) - M) * LOG2E);
      L = fmaf(wt, __uint_as_float(ml[s][2]), L);
      o[0] = fmaf(wt, bf2f(ov[s][0] & 0xffffu), o[0]);
      o[1] = fmaf(wt, bf2f(ov[s][0] >> 16), o[1]);
      o[2] = fmaf(wt, bf2f(ov[s][2] & 0xffffu), o[2]);
      o[3] = fmaf(wt, bf2f(ov[s][2] >> 16), o[3]);
    }
    const float inv = 1.f / L;
    u32* a3 = (u32*)(smem + L_A3) + b * (K3_STRIDE / 2) + 64 * hh + 2 * jj;
    a3[0] = f2bf(o[0] * inv) | (f2bf(o[1] * inv) << 16);
    a3[1] = f2bf(o[2] * inv) | (f2bf(o[3] * inv) << 16);
  }
  if (wave == 0) STAMP(12);
  vm_wait0();
  lds_wait();
  __builtin_amdgcn_s_barrier();
  if (wave == 0) STAMP(13);
  if (wave < 4) {
    f32x4 d = {0.f, 0.f, 0.f, 0.f};
#pragma unroll
    for (int ks = 0; ks < 23; ++ks) {
      bf16x8 a = {0, 0, 0, 0, 0, 0, 0, 0};
      if (m16 < 8) a = *(const bf16x8*)(smem + L_A3 + (m16 * K3_STRIDE + ks * 32 + g4 * 8) * 2);
      d = __builtin_amdgcn_mfma_f32_16x16x32_bf16(a, bfrag_tr(smem + L_W3, ks, wave, lane), d, 0, 0, 0);
    }
    if (lane < 32) {
      const int col = 16 * wave + m16;
      u64* og = p.ws + OG_OFF + ((((size_t)cg * 8 + (col >> 3)) * 8 + rg) * 8) * 8 + (col & 7);
#pragma unroll
      for (int i = 0; i < 4; ++i) put_gran(og + (4 * g4 + i) * 8, __float_as_uint(d[i]));
    }
  }
  if (wave == 0) STAMP(14);
  if (wave < 8) {
    const u64* base = p.ws + OG_OFF + (((size_t)cg * 8 + rg) * 8) * 64 + tid;
    u64 a = 0;
    for (u32 it = 0; it < SPIN_CAP; ++it) {
      a = get_gran(base);
      if (__all(tag_ok(a))) break;
      __builtin_amdgcn_s_sleep(1);
    }
    ((float*)(smem + L_RED))[tid] = __uint_as_float((u32)a);
  }
  if (wave == 0) STAMP(15);
  lds_wait();
  __builtin_amdgcn_s_barrier();
  if (tid < 64) {
    const float* red = (const float*)(smem + L_RED);
    float r = xres;
#pragma unroll
    for (int w = 0; w < 8; ++w) r += red[w * 64 + tid];
    p.out[(tid >> 3) * DM + ocol + (tid & 7)] = (u16)f2bf(r);
  }
  if (wave == 0) STAMP(16);
}

}

extern "C" void kernel_launch(void* const* d_in, const int* in_sizes, int n_in,
                              void* d_out, int out_size, void* d_ws, size_t ws_size,
                              hipStream_t stream) {
  Params p;
  p.x = (const u16*)d_in[0];
  p.kc = (const u16*)d_in[1];
  p.vc = (const u16*)d_in[2];
  p.wq = (const u16*)d_in[3];
  p.wo = (const u16*)d_in[4];
  p.w1 = (const u16*)d_in[6];
  p.b1 = (const u16*)d_in[7];
  p.w2 = (const u16*)d_in[8];
  p.b2 = (const u16*)d_in[9];
  p.out = (u16*)d_out;
  p.ws = (u64*)d_ws;
  (void)hipMemsetAsync(d_ws, 0, WS_BYTES, stream);
  hipLaunchKernelGGL(fused_decode, dim3(NWG), dim3(NTHR), 0, stream, p);
}
```

```cpp
#include <hip/hip_runtime.h>
#include <hip/hip_bf16.h>
#include <stdint.h>

typedef unsigned int u32;
typedef unsigned long long u64;
typedef unsigned short u16;
typedef u32 u32x4 __attribute__((ext_vector_type(4)));
typedef float f32x4 __attribute__((ext_vector_type(4)));
typedef short bf16x8 __attribute__((ext_vector_type(8)));
typedef short s16x4 __attribute__((ext_vector_type(4)));

#define GAS __attribute__((address_space(1)))
#define LAS __attribute__((address_space(3)))
#define RLX_AGENT __ATOMIC_RELAXED, __HIP_MEMORY_SCOPE_AGENT

namespace {

constexpr int NB = 8, DM = 512, HQ = 32, HKV = 8, SK = 4096, DH = 128, WINDOW = 1024, FF = 1792;
constexpr int KOFF = SK - WINDOW;
constexpr int NWG = 256, NTHR = 1024;
constexpr u32 TAG = 0x3C5A0001u;
constexpr u32 SPIN_CAP = 1u << 22;
constexpr float LOG2E = 1.4426950408889634f;

constexpr int NSP = 3;
constexpr int PREC = 66;
constexpr size_t QG_OFF = 0, QG_N = (size_t)NB * 2048;
constexpr size_t HG_OFF = QG_OFF + QG_N, HG_N = (size_t)NB * 2 * FF;
constexpr size_t PG_OFF = HG_OFF + HG_N, PG_N = (size_t)HQ * NB * NSP * PREC;
constexpr size_t OG_OFF = PG_OFF + PG_N, OG_N = (size_t)8 * 8 * 8 * 8 * 8;
constexpr size_t WS_GRAN = OG_OFF + OG_N;
constexpr size_t WS_BYTES = WS_GRAN * 8;
static_assert(WS_BYTES % 16 == 0, "memset block must be a multiple of 16 bytes");

constexpr int L_CTRL = 0;
constexpr int L_Q = 64;
constexpr int L_ML = L_Q + 1024;
constexpr int L_SC = L_ML + 512;
constexpr int L_PB = L_SC + 2048;
constexpr int L_V = L_PB + 11 * 1024;
constexpr int LA_END = L_V + 352 * 256;
constexpr int XN_STRIDE = 520;
constexpr int K3 = 736, K3_STRIDE = 744;
constexpr int L_XN = 64;
constexpr int L_A3 = L_XN + NB * XN_STRIDE * 2;
constexpr int L_RED = L_A3 + NB * K3_STRIDE * 2;
constexpr int L_WQ = L_RED + 2048;
constexpr int L_W1 = L_WQ + 65536;
constexpr int L_W3 = L_WQ;
constexpr int L_PD = L_W1 + 32768;
constexpr int LB_END = L_PD + 16384;
static_assert(L_W3 + K3 * 128 <= LB_END, "W3 overlay");
constexpr int LDS_BYTES = LA_END > LB_END ? LA_END : LB_END;
static_assert(LDS_BYTES > 80 * 1024 && LDS_BYTES <= 160 * 1024, "one workgroup per CU");
static_assert(L_WQ % 16 == 0 && L_A3 % 16 == 0 && L_V % 16 == 0 && L_PB % 16 == 0 && L_RED % 16 == 0, "align");

struct Params {
  const u16* x; const u16* kc; const u16* vc; const u16* wq; const u16* wo;
  const u16* w1; const u16* b1; const u16* w2; const u16* b2;
  u16* out; u64* ws;
};

__device__ __forceinline__ float bf2f(u32 h) { return __uint_as_float(h << 16); }
__device__ __forceinline__ u32 f2bf(float f) {
  __bf16 h = (__bf16)f;
  return (u32)__builtin_bit_cast(u16, h);
}
__device__ __forceinline__ float bfr(float f) { return bf2f(f2bf(f)); }

template <int CTRL> __device__ __forceinline__ float dpp(float x) {
  return __builtin_bit_cast(float, __builtin_amdgcn_mov_dpp(__builtin_bit_cast(int, x), CTRL, 0xf, 0xf, true));
}
template <int CTRL> __device__ __forceinline__ u32 dppu(u32 x) {
  return (u32)__builtin_amdgcn_mov_dpp((int)x, CTRL, 0xf, 0xf, true);
}
__device__ __forceinline__ float x16_max(float x) {
  auto s = __builtin_amdgcn_permlane16_swap(__float_as_uint(x), __float_as_uint(x), false, false);
  return fmaxf(__uint_as_float(s[0]), __uint_as_float(s[1]));
}
__device__ __forceinline__ float x32_max(float x) {
  auto t = __builtin_amdgcn_permlane32_swap(__float_as_uint(x), __float_as_uint(x), false, false);
  return fmaxf(__uint_as_float(t[0]), __uint_as_float(t[1]));
}
__device__ __forceinline__ float x16_sum(float x) {
  auto s = __builtin_amdgcn_permlane16_swap(__float_as_uint(x), __float_as_uint(x), false, false);
  return __uint_as_float(s[0]) + __uint_as_float(s[1]);
}
__device__ __forceinline__ float x32_sum(float x) {
  auto t = __builtin_amdgcn_permlane32_swap(__float_as_uint(x), __float_as_uint(x), false, false);
  return __uint_as_float(t[0]) + __uint_as_float(t[1]);
}

__device__ __forceinline__ void put_gran(u64* g, u32 v) {
  __hip_atomic_store((GAS u64*)g, ((u64)TAG << 32) | (u64)v, RLX_AGENT);
}
__device__ __forceinline__ u64 get_gran(const u64* g) {
  return __hip_atomic_load((GAS u64*)g, RLX_AGENT);
}
__device__ __forceinline__ bool tag_ok(u64 v) { return (u32)(v >> 32) == TAG; }
__device__ __forceinline__ u32x4 get_gran2(__amdgpu_buffer_rsrc_t rs, size_t gidx) {
  return __builtin_amdgcn_raw_buffer_load_b128(rs, (int)(gidx * 8), 0, 16);
}

__device__ __forceinline__ void lds_wait() { asm volatile("s_waitcnt lgkmcnt(0)" ::: "memory"); }
__device__ __forceinline__ void vm_wait0() { asm volatile("s_waitcnt vmcnt(0)" ::: "memory"); }

typedef volatile LAS u32 lctr;
__device__ __forceinline__ void lds_add1(lctr* c) {
  __hip_atomic_fetch_add((LAS u32*)c, 1u, __ATOMIC_RELAXED, __HIP_MEMORY_SCOPE_WORKGROUP);
}
__device__ __forceinline__ void lds_arrive(lctr* c, int lane) {
  lds_wait();
  if (lane == 0) lds_add1(c);
  asm volatile("" ::: "memory");
}
__device__ __forceinline__ void lds_spin(lctr* c, u32 target) {
  for (u32 it = 0; it < SPIN_CAP; ++it) {
    if (*c >= target) break;
    __builtin_amdgcn_s_sleep(1);
  }
  asm volatile("" ::: "memory");
}

__device__ __forceinline__ float gelu_tanh(float v) {
  const float u = 0.7978845608028654f * (v + 0.044715f * v * v * v);
  return 0.5f * v * (1.f + tanhf(u));
}

__device__ __forceinline__ void dma16(const void* gsrc, unsigned char* lds_base) {
  __builtin_amdgcn_global_load_lds(gsrc, (LAS void*)lds_base, 16, 0, 2);
}
__device__ __forceinline__ bf16x8 bfrag_tr(const unsigned char* tile, int ks, int nt, int lane) {
  const int g = lane >> 4, q = (lane & 15) >> 2, pp = lane & 3;
  const unsigned char* a = tile + (32 * ks + 8 * g + q) * 128 + (16 * nt + 4 * pp) * 2;
  const s16x4 lo = __builtin_amdgcn_ds_read_tr16_b64_v4i16((LAS s16x4*)a);
  const s16x4 hi = __builtin_amdgcn_ds_read_tr16_b64_v4i16((LAS s16x4*)(a + 4 * 128));
  return bf16x8{lo[0], lo[1], lo[2], lo[3], hi[0], hi[1], hi[2], hi[3]};
}
__device__ __forceinline__ int vswz(int row) { return ((row & 3) << 2) | ((row >> 2) & 3); }

#define KV_WIN 5
#define Q_SLEEP 8
#define PG_SLEEP 4
#define STAMP(k) do { } while (0)

__global__ void __launch_bounds__(NTHR, 4) fused_decode(Params p) {
  __shared__ __attribute__((aligned(16))) unsigned char smem[LDS_BYTES];
  lctr* ctrl = (lctr*)(smem + L_CTRL);

  const int tid = threadIdx.x, lane = tid & 63;
  const int wave = __builtin_amdgcn_readfirstlane(tid >> 6);
  const int bid = blockIdx.x, xg = bid & 7, y = bid >> 3;
  const int g4 = lane >> 4, m16 = lane & 15;

  if (y < 24) {
    const int hkv = xg, bb = y / 3, sp = y - 3 * bb;
    const int nkw = sp == 2 ? 10 : 11;
    if (wave > 11 || (wave == 10 && nkw == 10)) return;
    if (wave == 11) {
      if (lane < 16) ctrl[lane] = 0u;
      lds_wait();
      __builtin_amdgcn_s_barrier();
      STAMP(6);
      const u64* g = p.ws + QG_OFF + (size_t)bb * 2048 + hkv * 256 + 4 * lane;
      u64 v[4];
      for (u32 it = 0; it < SPIN_CAP; ++it) {
#pragma unroll
        for (int k = 0; k < 4; ++k) v[k] = get_gran(g + k);
        if (__all(tag_ok(v[0]) && tag_ok(v[1]) && tag_ok(v[2]) && tag_ok(v[3]))) break;
        __builtin_amdgcn_s_sleep(Q_SLEEP);
      }
      *(u32x4*)(smem + L_Q + lane * 16) = u32x4{(u32)v[0], (u32)v[1], (u32)v[2], (u32)v[3]};
      lds_arrive(&ctrl[0], lane);
      STAMP(7);
      return;
    }
    if (wave == 0) STAMP(0);
    const size_t kvbase = ((size_t)(bb * HKV + hkv) * SK + KOFF + sp * 352 + wave * 32) * DH;
    const u32x4* kp = (const u32x4*)(p.kc + kvbase + (size_t)m16 * DH + g4 * 8);
    u32x4 kf[8];
#define KV_ISSUE(n) do { \
      if ((n) < 8) { kf[(n) & 7] = __builtin_nontemporal_load(kp + ((n) >> 2) * 16 * (DH / 8) + ((n) & 3) * 4); } \
      else { const int row_ = 32 * wave + 4 * ((n) - 8) + g4; \
             dma16(p.vc + kvbase + (size_t)(4 * ((n) - 8) + g4) * DH + ((m16 ^ vswz(row_)) * 8), smem + L_V + (32 * wave + 4 * ((n) - 8)) * 256); } \
      asm volatile("" ::: "memory"); } while (0)
#pragma unroll
    for (int n = 0; n < KV_WIN; ++n) KV_ISSUE(n);
    __builtin_amdgcn_s_barrier();
#pragma unroll
    for (int n = KV_WIN; n < 16; ++n) {
      asm volatile("s_waitcnt vmcnt(%0)" :: "i"(KV_WIN - 1) : "memory");
      KV_ISSUE(n);
    }
#undef KV_ISSUE
    if (wave == 0) STAMP(1);
    lds_spin(&ctrl[0], 1);
    if (wave == 0) STAMP(2);
    bf16x8 qf[4];
#pragma unroll
    for (int t = 0; t < 4; ++t) {
      bf16x8 z = {0, 0, 0, 0, 0, 0, 0, 0};
      if (m16 < 4) z = *(const bf16x8*)(smem + L_Q + (m16 * 128 + t * 32 + g4 * 8) * 2);
      qf[t] = z;
    }
    constexpr float SCALE = 0.08838834764831845f;
    float a[8];
#pragma unroll
    for (int T = 0; T < 2; ++T) {
      f32x4 s = {0.f, 0.f, 0.f, 0.f};
#pragma unroll
      for (int t = 0; t < 4; ++t) s = __builtin_amdgcn_mfma_f32_16x16x32_bf16(__builtin_bit_cast(bf16x8, kf[4 * T + t]), qf[t], s, 0, 0, 0);
#pragma unroll
      for (int i = 0; i < 4; ++i) a[4 * T + i] = bfr(bfr(s[i]) * SCALE);
    }
    float m = a[0];
#pragma unroll
    for (int i = 1; i < 8; ++i) m = fmaxf(m, a[i]);
    m = x32_max(x16_max(m));
    float l = 0.f;
    u32x4 pf;
#pragma unroll
    for (int i = 0; i < 4; ++i) {
      const u32 lo = f2bf(__builtin_amdgcn_exp2f((a[2 * i] - m) * LOG2E)), hi = f2bf(__builtin_amdgcn_exp2f((a[2 * i + 1] - m) * LOG2E));
      pf[i] = lo | (hi << 16);
      l += bf2f(lo) + bf2f(hi);
    }
    l = x32_sum(x16_sum(l));
    *(u32x4*)(smem + L_PB + (wave * 64 + lane) * 16) = pf;
    if (lane < 4) {
      float* ml = (float*)(smem + L_ML) + (wave * 4 + lane) * 2;
      ml[0] = m; ml[1] = l;
    }
    if (wave == 0) STAMP(3);
    vm_wait0();
    lds_arrive(&ctrl[1], lane);
    if (wave >= 8) return;
    lds_spin(&ctrl[1], (u32)nkw);
    if (wave == 0) STAMP(4);
    float Mx, Lx;
    {
      const int ws = lane >> 2, h = lane & 3;
      const bool live = ws < nkw;
      const float* ml = (const float*)(smem + L_ML) + ((live ? ws : 0) * 4 + h) * 2;
      const float mw = live ? ml[0] : -INFINITY, lw = live ? ml[1] : 0.f;
      float M = mw;
      M = fmaxf(M, dpp<0x124>(M)); M = fmaxf(M, dpp<0x128>(M)); M = x32_max(x16_max(M));
      const float sc = live ? __builtin_amdgcn_exp2f((mw - M) * LOG2E) : 0.f;
      float L = sc * lw;
      L += dpp<0x124>(L); L += dpp<0x128>(L); L = x32_sum(x16_sum(L));
      Mx = M; Lx = L;
      ((float*)(smem + L_SC))[wave * 64 + lane] = sc;
      lds_wait();
      __builtin_amdgcn_wave_barrier();
    }
    f32x4 o = {0.f, 0.f, 0.f, 0.f};
    {
      const int q = m16 >> 2, pp = lane & 3;
      const int ch = 2 * wave + (pp >> 1);
#pragma unroll
      for (int w = 0; w < 11; ++w) {
        if (w < 10 || nkw == 11) {
          const bf16x8 pa = *(const bf16x8*)(smem + L_PB + (w * 64 + lane) * 16);
          const int r0 = 32 * w + 4 * g4 + q, r1 = r0 + 16;
          const s16x4 lo = __builtin_amdgcn_ds_read_tr16_b64_v4i16((LAS s16x4*)(smem + L_V + r0 * 256 + ((ch ^ vswz(r0)) * 16) + 8 * (pp & 1)));
          const s16x4 hi = __builtin_amdgcn_ds_read_tr16_b64_v4i16((LAS s16x4*)(smem + L_V + r1 * 256 + ((ch ^ vswz(r1)) * 16) + 8 * (pp & 1)));
          const bf16x8 vb = {lo[0], lo[1], lo[2], lo[3], hi[0], hi[1], hi[2], hi[3]};
          const f32x4 z = {0.f, 0.f, 0.f, 0.f};
          const f32x4 d = __builtin_amdgcn_mfma_f32_16x16x32_bf16(pa, vb, z, 0, 0, 0);
          const f32x4 sc = *(const f32x4*)(smem + L_SC + (wave * 64 + w * 4) * 4);
#pragma unroll
          for (int i = 0; i < 4; ++i) o[i] = fmaf(sc[i], d[i], o[i]);
        }
      }
    }
    {
      u64* rec = p.ws + PG_OFF + (((size_t)(hkv * 4) * NB + bb) * NSP + sp) * PREC;
#pragma unroll
      for (int h = 0; h < 4; ++h) {
        const u32 mine = f2bf(o[h]);
        const u32 other = dppu<0xB1>(mine);
        if (lane < 16 && (lane & 1) == 0) put_gran(rec + (size_t)h * NB * NSP * PREC + 8 * wave + (lane >> 1), mine | (other << 16));
      }
      if (wave == 0 && lane < 4) {
        put_gran(rec + (size_t)lane * NB * NSP * PREC + 64, __float_as_uint(Mx));
        put_gran(rec + (size_t)lane * NB * NSP * PREC + 65, __float_as_uint(Lx));
      }
    }
    if (wave == 0) STAMP(5);
    return;
  }

  const int j = xg + 8 * (y - 24);
  if (wave == 0) STAMP(8);
  if (tid < 16) ctrl[tid] = 0u;
  const bool has_ffn = j < 56;
  const int fcb = j % 28, fks = j / 28;
  const int rg = j >> 3, cg = j & 7;

  const int ocol = 64 * cg + 8 * rg;
  u32 xr0 = 0, xr1 = 0;
  if (tid < 64) { xr0 = p.x[(tid >> 3) * DM + ocol + (tid & 7)]; xr1 = p.b2[ocol + (tid & 7)]; }
  u32 bias = 0;
  if (tid < 896) bias = *(const u32*)(p.b1 + 224 * rg + 2 * (tid % 112));
  u32x4 xa = {0, 0, 0, 0};
  if (wave < 8) xa = *(const u32x4*)(p.x + wave * DM + lane * 8);
#pragma unroll
  for (int i = 0; i < 4; ++i) {
    const int r0 = 128 * i + 8 * wave;
    dma16(p.wq + (size_t)(r0 + (lane >> 3)) * 4096 + 64 * j + 8 * (lane & 7), smem + L_WQ + r0 * 128);
  }
  if (has_ffn) {
#pragma unroll
    for (int i = 0; i < 2; ++i) {
      const int r0 = 128 * i + 8 * wave;
      dma16(p.w1 + (size_t)(256 * fks + r0 + (lane >> 3)) * FF + 64 * fcb + 8 * (lane & 7), smem + L_W1 + r0 * 128);
    }
  }
  if (wave < 8) {
    float xv[8];
#pragma unroll
    for (int k = 0; k < 4; ++k) { xv[2 * k] = __uint_as_float(xa[k] << 16); xv[2 * k + 1] = __uint_as_float(xa[k] & 0xffff0000u); }
    float s = 0.f;
#pragma unroll
    for (int k = 0; k < 8; ++k) s += xv[k];
    s += dpp<0x121>(s); s += dpp<0x122>(s); s += dpp<0x124>(s); s += dpp<0x128>(s); s = x32_sum(x16_sum(s));
    const float mean = s * (1.f / 512.f);
    float v = 0.f;
#pragma unroll
    for (int k = 0; k < 8; ++k) { const float d = xv[k] - mean; v = fmaf(d, d, v); }
    v += dpp<0x121>(v); v += dpp<0x122>(v); v += dpp<0x124>(v); v += dpp<0x128>(v); v = x32_sum(x16_sum(v));
    const float mb = bfr(mean), vb = bfr(v * (1.f / 512.f));
    const float den = bfr(sqrtf(bfr(vb + 1e-5f)));
    u32x4 o;
#pragma unroll
    for (int k = 0; k < 4; ++k) {
      const float a = bfr(xv[2 * k] - mb) / den, b = bfr(xv[2 * k + 1] - mb) / den;
      o[k] = f2bf(a) | (f2bf(b) << 16);
    }
    *(u32x4*)(smem + L_XN + (wave * XN_STRIDE + lane * 8) * 2) = o;
  }
  vm_wait0();
  lds_wait();
  __builtin_amdgcn_s_barrier();
  if (wave == 0) STAMP(9);
  {
    const int nt = wave & 3, kq = wave >> 2;
    bf16x8 af[4], bq[4], ag[2], bg[2];
#pragma unroll
    for (int k = 0; k < 4; ++k) {
      bf16x8 a = {0, 0, 0, 0, 0, 0, 0, 0};
      if (m16 < 8) a = *(const bf16x8*)(smem + L_XN + (m16 * XN_STRIDE + (4 * kq + k) * 32 + g4 * 8) * 2);
      af[k] = a;
      bq[k] = bfrag_tr(smem + L_WQ, 4 * kq + k, nt, lane);
    }
    if (has_ffn) {
#pragma unroll
      for (int k = 0; k < 2; ++k) {
        bf16x8 a = {0, 0, 0, 0, 0, 0, 0, 0};
        if (m16 < 8) a = *(const bf16x8*)(smem + L_XN + (m16 * XN_STRIDE + 256 * fks + (2 * kq + k) * 32 + g4 * 8) * 2);
        ag[k] = a;
        bg[k] = bfrag_tr(smem + L_W1, 2 * kq + k, nt, lane);
      }
    }
    f32x4 d = {0.f, 0.f, 0.f, 0.f};
#pragma unroll
    for (int k = 0; k < 4; ++k) d = __builtin_amdgcn_mfma_f32_16x16x32_bf16(af[k], bq[k], d, 0, 0, 0);
    if (lane < 32) *(f32x4*)(smem + L_PD + ((kq * 4 + nt) * 32 + lane) * 16) = d;
    if (has_ffn) {
      f32x4 e = {0.f, 0.f, 0.f, 0.f};
#pragma unroll
      for (int k = 0; k < 2; ++k) e = __builtin_amdgcn_mfma_f32_16x16x32_bf16(ag[k], bg[k], e, 0, 0, 0);
      if (lane < 32) *(f32x4*)(smem + L_PD + 8192 + ((kq * 4 + nt) * 32 + lane) * 16) = e;
    }
  }
  lds_wait();
  __builtin_amdgcn_s_barrier();
  if (wave < 4) {
    f32x4 d = {0.f, 0.f, 0.f, 0.f};
    if (lane < 32) {
#pragma unroll
      for (int kq = 0; kq < 4; ++kq) d += *(const f32x4*)(smem + L_PD + ((kq * 4 + wave) * 32 + lane) * 16);
    }
#pragma unroll
    for (int i = 0; i < 4; ++i) {
      const u32 mine = f2bf(d[i]);
      const u32 other = dppu<0xB1>(mine);
      if (lane < 32 && (lane & 1) == 0)
        put_gran(p.ws + QG_OFF + (size_t)(4 * g4 + i) * 2048 + 32 * j + 8 * wave + (m16 >> 1), mine | (other << 16));
    }
  } else if (wave < 8 && has_ffn) {
    if (lane < 32) {
      f32x4 e = {0.f, 0.f, 0.f, 0.f};
#pragma unroll
      for (int kq = 0; kq < 4; ++kq) e += *(const f32x4*)(smem + L_PD + 8192 + ((kq * 4 + wave - 4) * 32 + lane) * 16);
#pragma unroll
      for (int i = 0; i < 4; ++i)
        put_gran(p.ws + HG_OFF + ((size_t)(4 * g4 + i) * 2 + fks) * FF + 64 * fcb + 16 * (wave - 4) + m16, __float_as_uint(e[i]));
    }
  }
  if (wave == 0) STAMP(10);
  if (wave >= 14) {
#pragma unroll
    for (int i = 0; i < 46; ++i) {
      const int r0 = 16 * i + 8 * (wave - 14);
      const int r = r0 + (lane >> 3);
      const u16* src = r0 < 512 ? (p.wo + (size_t)(512 * rg + r) * DM) : (p.w2 + (size_t)(224 * rg + r - 512) * DM);
      dma16(src + 64 * cg + 8 * (lane & 7), smem + L_W3 + r0 * 128);
    }
    vm_wait0();
    lds_arrive(&ctrl[5], lane);
  }
  if (tid < 896) {
    const int b = tid / 112, jj = tid - 112 * b;
    const u64* g = p.ws + HG_OFF + (size_t)b * 2 * FF + 224 * rg + 2 * jj;
    u64 v[4];
    for (u32 it = 0; it < SPIN_CAP; ++it) {
      v[0] = get_gran(g); v[1] = get_gran(g + 1); v[2] = get_gran(g + FF); v[3] = get_gran(g + FF + 1);
      if (tag_ok(v[0]) && tag_ok(v[1]) && tag_ok(v[2]) && tag_ok(v[3])) break;
      __builtin_amdgcn_s_sleep(4);
    }
    float h0 = __uint_as_float((u32)v[0]) + __uint_as_float((u32)v[2]);
    float h1 = __uint_as_float((u32)v[1]) + __uint_as_float((u32)v[3]);
    h0 = gelu_tanh(bfr(bfr(h0) + bf2f(bias & 0xffffu)));
    h1 = gelu_tanh(bfr(bfr(h1) + bf2f(bias >> 16)));
    ((u32*)(smem + L_A3))[b * (K3_STRIDE / 2) + 256 + jj] = f2bf(h0) | (f2bf(h1) << 16);
  }
  lds_arrive(&ctrl[6], lane);
  lds_spin(&ctrl[6], 16);
  lds_spin(&ctrl[5], 2);
  f32x4 dffn = {0.f, 0.f, 0.f, 0.f};
  bf16x8 b3[4];
  {
    const int nt = wave & 3, hh = wave >> 2;
#pragma unroll
    for (int k = 0; k < 4; ++k) b3[k] = bfrag_tr(smem + L_W3, 4 * hh + k, nt, lane);
    if (wave < 4) {
#pragma unroll
      for (int ks = 16; ks < 23; ++ks) {
        bf16x8 a = {0, 0, 0, 0, 0, 0, 0, 0};
        if (m16 < 8) a = *(const bf16x8*)(smem + L_A3 + (m16 * K3_STRIDE + ks * 32 + g4 * 8) * 2);
        dffn = __builtin_amdgcn_mfma_f32_16x16x32_bf16(a, bfrag_tr(smem + L_W3, ks, nt, lane), dffn, 0, 0, 0);
      }
    }
  }
  if (wave == 0) STAMP(11);
  if (wave == 13) {
    const u64* base = p.ws + PG_OFF + (size_t)(4 * rg) * NB * NSP * PREC + 65;
    for (u32 it = 0; it < SPIN_CAP; ++it) {
      const u64 a = get_gran(base + (size_t)lane * PREC);
      u64 b = 0;
      if (lane < 32) b = get_gran(base + (size_t)(64 + lane) * PREC);
      if (__all(tag_ok(a) && (lane >= 32 || tag_ok(b)))) break;
      __builtin_amdgcn_s_sleep(PG_SLEEP);
    }
    lds_arrive(&ctrl[4], lane);
  } else {
    lds_spin(&ctrl[4], 1);
  }
  {
    const int hh = tid >> 8, b = (tid >> 5) & 7, jj = tid & 31;
    const size_t base = PG_OFF + ((size_t)(4 * rg + hh) * NB + b) * NSP * PREC;
    const __amdgpu_buffer_rsrc_t rs = __builtin_amdgcn_make_buffer_rsrc((void*)p.ws, 0, (int)WS_BYTES, 0x00020000);
    u32x4 ov[NSP], ml[NSP];
    for (u32 it = 0; it < SPIN_CAP; ++it) {
      bool ok = true;
#pragma unroll
      for (int s = 0; s < NSP; ++s) {
        ov[s] = get_gran2(rs, base + s * PREC + 2 * jj);
        ml[s] = get_gran2(rs, base + s * PREC + 64);
      }
#pragma unroll
      for (int s = 0; s < NSP; ++s) ok = ok && ov[s][1] == TAG && ov[s][3] == TAG && ml[s][1] == TAG && ml[s][3] == TAG;
      if (__all(ok)) break;
      __builtin_amdgcn_s_sleep(1);
    }
    float M = -INFINITY;
#pragma unroll
    for (int s = 0; s < NSP; ++s) M = fmaxf(M, __uint_as_float(ml[s][0]));
    float L = 0.f, o[4] = {0.f, 0.f, 0.f, 0.f};
#pragma unroll
    for (int s = 0; s < NSP; ++s) {
      const float wt = __builtin_amdgcn_exp2f((__uint_as_float(ml[s][0]) - M) * LOG2E);
      L = fmaf(wt, __uint_as_float(ml[s][2]), L);
      o[0] = fmaf(wt, bf2f(ov[s][0] & 0xffffu), o[0]);
      o[1] = fmaf(wt, bf2f(ov[s][0] >> 16), o[1]);
      o[2] = fmaf(wt, bf2f(ov[s][2] & 0xffffu), o[2]);
      o[3] = fmaf(wt, bf2f(ov[s][2] >> 16), o[3]);
    }
    const float inv = 1.f / L;
    u32* a3 = (u32*)(smem + L_A3) + b * (K3_STRIDE / 2) + 64 * hh + 2 * jj;
    a3[0] = f2bf(o[0] * inv) | (f2bf(o[1] * inv) << 16);
    a3[1] = f2bf(o[2] * inv) | (f2bf(o[3] * inv) << 16);
  }
  if (wave == 0) STAMP(12);
  lds_wait();
  __builtin_amdgcn_s_barrier();
  if (wave == 0) STAMP(13);
  {
    const int nt = wave & 3, hh = wave >> 2;
    bf16x8 af[4];
#pragma unroll
    for (int k = 0; k < 4; ++k) {
      bf16x8 a = {0, 0, 0, 0, 0, 0, 0, 0};
      if (m16 < 8) a = *(const bf16x8*)(smem + L_A3 + (m16 * K3_STRIDE + (4 * hh + k) * 32 + g4 * 8) * 2);
      af[k] = a;
    }
    f32x4 d = {0.f, 0.f, 0.f, 0.f};
#pragma unroll
    for (int k = 0; k < 4; ++k) d = __builtin_amdgcn_mfma_f32_16x16x32_bf16(af[k], b3[k], d, 0, 0, 0);
    if (lane < 32) *(f32x4*)(smem + L_PD + ((hh * 4 + nt) * 32 + lane) * 16) = d;
  }
  lds_wait();
  __builtin_amdgcn_s_barrier();
  if (wave < 4) {
    if (lane < 32) {
      f32x4 d = dffn;
#pragma unroll
      for (int hh = 0; hh < 4; ++hh) d += *(const f32x4*)(smem + L_PD + ((hh * 4 + wave) * 32 + lane) * 16);
      const int col = 16 * wave + m16;
      u64* og = p.ws + OG_OFF + ((((size_t)cg * 8 + (col >> 3)) * 8 + rg) * 8) * 8 + (col & 7);
#pragma unroll
      for (int i = 0; i < 4; ++i) put_gran(og + (4 * g4 + i) * 8, __float_as_uint(d[i]));
    }
  }
  if (wave == 0) STAMP(14);
  if (wave == 0) {
    const u64* base = p.ws + OG_OFF + (((size_t)cg * 8 + rg) * 8) * 64 + lane;
    u64 a[8];
    for (u32 it = 0; it < SPIN_CAP; ++it) {
      bool ok = true;
#pragma unroll
      for (int w = 0; w < 8; ++w) a[w] = get_gran(base + w * 64);
#pragma unroll
      for (int w = 0; w < 8; ++w) ok = ok && tag_ok(a[w]);
      if (__all(ok)) break;
      __builtin_amdgcn_s_sleep(1);
    }
    STAMP(15);
    float r = bf2f(xr0) + bf2f(xr1);
#pragma unroll
    for (int w = 0; w < 8; ++w) r += __uint_as_float((u32)a[w]);
    p.out[(lane >> 3) * DM + ocol + (lane & 7)] = (u16)f2bf(r);
    STAMP(16);
  }
}

}

extern "C" void kernel_launch(void* const* d_in, const int* in_sizes, int n_in,
                              void* d_out, int out_size, void* d_ws, size_t ws_size,
                              hipStream_t stream) {
  Params p;
  p.x = (const u16*)d_in[0];
  p.kc = (const u16*)d_in[1];
  p.vc = (const u16*)d_in[2];
  p.wq = (const u16*)d_in[3];
  p.wo = (const u16*)d_in[4];
  p.w1 = (const u16*)d_in[6];
  p.b1 = (const u16*)d_in[7];
  p.w2 = (const u16*)d_in[8];
  p.b2 = (const u16*)d_in[9];
  p.out = (u16*)d_out;
  p.ws = (u64*)d_ws;
  (void)hipMemsetAsync(d_ws, 0, WS_BYTES, stream);
  hipLaunchKernelGGL(fused_decode, dim3(NWG), dim3(NTHR), 0, stream, p);
}
```

```cpp
#include <hip/hip_runtime.h>
#include <hip/hip_bf16.h>
#include <stdint.h>

typedef unsigned int u32;
typedef unsigned long long u64;
typedef unsigned short u16;
typedef u32 u32x4 __attribute__((ext_vector_type(4)));
typedef float f32x4 __attribute__((ext_vector_type(4)));
typedef short bf16x8 __attribute__((ext_vector_type(8)));
typedef short s16x4 __attribute__((ext_vector_type(4)));

#define GAS __attribute__((address_space(1)))
#define LAS __attribute__((address_space(3)))
#define RLX_AGENT __ATOMIC_RELAXED, __HIP_MEMORY_SCOPE_AGENT

namespace {

constexpr int NB = 8, DM = 512, HQ = 32, HKV = 8, SK = 4096, DH = 128, WINDOW = 1024, FF = 1792;
constexpr int KOFF = SK - WINDOW;
constexpr int NWG = 256, NTHR = 1024;
constexpr u32 TAG = 0x3C5A0001u;
constexpr u32 SPIN_CAP = 1u << 22;
constexpr float LOG2E = 1.4426950408889634f;

constexpr int NSP = 3;
constexpr int PREC = 66;
constexpr size_t QG_OFF = 0, QG_N = (size_t)NB * 2048;
constexpr size_t HG_OFF = QG_OFF + QG_N, HG_N = (size_t)NB * 2 * FF;
constexpr size_t PG_OFF = HG_OFF + HG_N, PG_N = (size_t)HQ * NB * NSP * PREC;
constexpr size_t OG_OFF = PG_OFF + PG_N, OG_N = (size_t)8 * 8 * 8 * 8 * 8;
constexpr size_t WS_GRAN = OG_OFF + OG_N;
constexpr size_t WS_BYTES = WS_GRAN * 8;
static_assert(WS_BYTES % 16 == 0, "memset block must be a multiple of 16 bytes");

constexpr int L_CTRL = 0;
constexpr int L_Q = 64;
constexpr int L_ML = L_Q + 1024;
constexpr int L_SC = L_ML + 512;
constexpr int L_PB = L_SC + 2048;
constexpr int L_V = L_PB + 11 * 1024;
constexpr int LA_END = L_V + 352 * 256;
constexpr int XN_STRIDE = 520;
constexpr int K3 = 736, K3_STRIDE = 744;
constexpr int L_XN = 64;
constexpr int L_A3 = L_XN + NB * XN_STRIDE * 2;
constexpr int L_RED = L_A3 + NB * K3_STRIDE * 2;
constexpr int L_WQ = L_RED + 2048;
constexpr int L_W1 = L_WQ + 65536;
constexpr int L_W3 = L_WQ;
constexpr int L_PD = L_W1 + 32768;
constexpr int LB_END = L_PD + 16384;
static_assert(L_W3 + K3 * 128 <= LB_END, "W3 overlay");
constexpr int LDS_BYTES = LA_END > LB_END ? LA_END : LB_END;
static_assert(LDS_BYTES > 80 * 1024 && LDS_BYTES <= 160 * 1024, "one workgroup per CU");
static_assert(L_WQ % 16 == 0 && L_A3 % 16 == 0 && L_V % 16 == 0 && L_PB % 16 == 0 && L_RED % 16 == 0, "align");

struct Params {
  const u16* x; const u16* kc; const u16* vc; const u16* wq; const u16* wo;
  const u16* w1; const u16* b1; const u16* w2; const u16* b2;
  u16* out; u64* ws;
};

__device__ __forceinline__ float bf2f(u32 h) { return __uint_as_float(h << 16); }
__device__ __forceinline__ u32 f2bf(float f) {
  __bf16 h = (__bf16)f;
  return (u32)__builtin_bit_cast(u16, h);
}
__device__ __forceinline__ float bfr(float f) { return bf2f(f2bf(f)); }

template <int CTRL> __device__ __forceinline__ float dpp(float x) {
  return __builtin_bit_cast(float, __builtin_amdgcn_mov_dpp(__builtin_bit_cast(int, x), CTRL, 0xf, 0xf, true));
}
template <int CTRL> __device__ __forceinline__ u32 dppu(u32 x) {
  return (u32)__builtin_amdgcn_mov_dpp((int)x, CTRL, 0xf, 0xf, true);
}
__device__ __forceinline__ float x16_max(float x) {
  auto s = __builtin_amdgcn_permlane16_swap(__float_as_uint(x), __float_as_uint(x), false, false);
  return fmaxf(__uint_as_float(s[0]), __uint_as_float(s[1]));
}
__device__ __forceinline__ float x32_max(float x) {
  auto t = __builtin_amdgcn_permlane32_swap(__float_as_uint(x), __float_as_uint(x), false, false);
  return fmaxf(__uint_as_float(t[0]), __uint_as_float(t[1]));
}
__device__ __forceinline__ float x16_sum(float x) {
  auto s = __builtin_amdgcn_permlane16_swap(__float_as_uint(x), __float_as_uint(x), false, false);
  return __uint_as_float(s[0]) + __uint_as_float(s[1]);
}
__device__ __forceinline__ float x32_sum(float x) {
  auto t = __builtin_amdgcn_permlane32_swap(__float_as_uint(x), __float_as_uint(x), false, false);
  return __uint_as_float(t[0]) + __uint_as_float(t[1]);
}

__device__ __forceinline__ void put_gran(u64* g, u32 v) {
  __hip_atomic_store((GAS u64*)g, ((u64)TAG << 32) | (u64)v, RLX_AGENT);
}
__device__ __forceinline__ u64 get_gran(const u64* g) {
  return __hip_atomic_load((GAS u64*)g, RLX_AGENT);
}
__device__ __forceinline__ bool tag_ok(u64 v) { return (u32)(v >> 32) == TAG; }
__device__ __forceinline__ u32x4 get_gran2(__amdgpu_buffer_rsrc_t rs, size_t gidx) {
  return __builtin_amdgcn_raw_buffer_load_b128(rs, (int)(gidx * 8), 0, 16);
}

__device__ __forceinline__ void lds_wait() { asm volatile("s_waitcnt lgkmcnt(0)" ::: "memory"); }
__device__ __forceinline__ void vm_wait0() { asm volatile("s_waitcnt vmcnt(0)" ::: "memory"); }

typedef volatile LAS u32 lctr;
__device__ __forceinline__ void lds_add1(lctr* c) {
  __hip_atomic_fetch_add((LAS u32*)c, 1u, __ATOMIC_RELAXED, __HIP_MEMORY_SCOPE_WORKGROUP);
}
__device__ __forceinline__ void lds_arrive(lctr* c, int lane) {
  lds_wait();
  if (lane == 0) lds_add1(c);
  asm volatile("" ::: "memory");
}
__device__ __forceinline__ void lds_spin(lctr* c, u32 target) {
  for (u32 it = 0; it < SPIN_CAP; ++it) {
    if (*c >= target) break;
    __builtin_amdgcn_s_sleep(1);
  }
  asm volatile("" ::: "memory");
}

__device__ __forceinline__ float gelu_tanh(float v) {
  const float u = 0.7978845608028654f * (v + 0.044715f * v * v * v);
  return 0.5f * v * (1.f + tanhf(u));
}

__device__ __forceinline__ void dma16(const void* gsrc, unsigned char* lds_base) {
  __builtin_amdgcn_global_load_lds(gsrc, (LAS void*)lds_base, 16, 0, 2);
}
__device__ __forceinline__ bf16x8 bfrag_tr(const unsigned char* tile, int ks, int nt, int lane) {
  const int g = lane >> 4, q = (lane & 15) >> 2, pp = lane & 3;
  const unsigned char* a = tile + (32 * ks + 8 * g + q) * 128 + (16 * nt + 4 * pp) * 2;
  const s16x4 lo = __builtin_amdgcn_ds_read_tr16_b64_v4i16((LAS s16x4*)a);
  const s16x4 hi = __builtin_amdgcn_ds_read_tr16_b64_v4i16((LAS s16x4*)(a + 4 * 128));
  return bf16x8{lo[0], lo[1], lo[2], lo[3], hi[0], hi[1], hi[2], hi[3]};
}
__device__ __forceinline__ int vswz(int row) { return ((row & 3) << 2) | ((row >> 2) & 3); }

#define KV_WIN 5
#define Q_SLEEP 8
#define PG_SLEEP 4
#define STAMP(k) do { } while (0)

__global__ void __launch_bounds__(NTHR, 4) fused_decode(Params p) {
  __shared__ __attribute__((aligned(16))) unsigned char smem[LDS_BYTES];
  lctr* ctrl = (lctr*)(smem + L_CTRL);

  const int tid = threadIdx.x, lane = tid & 63;
  const int wave = __builtin_amdgcn_readfirstlane(tid >> 6);
  const int bid = blockIdx.x, xg = bid & 7, y = bid >> 3;
  const int g4 = lane >> 4, m16 = lane & 15;

  if (y < 24) {
    const int hkv = xg, bb = y / 3, sp = y - 3 * bb;
    const int nkw = sp == 2 ? 10 : 11;
    if (wave > 11 || (wave == 10 && nkw == 10)) return;
    if (wave == 11) {
      if (lane < 16) ctrl[lane] = 0u;
      lds_wait();
      __builtin_amdgcn_s_barrier();
      STAMP(6);
      const u64* g = p.ws + QG_OFF + (size_t)bb * 2048 + hkv * 256 + 4 * lane;
      u64 v[4];
      for (u32 it = 0; it < SPIN_CAP; ++it) {
#pragma unroll
        for (int k = 0; k < 4; ++k) v[k] = get_gran(g + k);
        if (__all(tag_ok(v[0]) && tag_ok(v[1]) && tag_ok(v[2]) && tag_ok(v[3]))) break;
        __builtin_amdgcn_s_sleep(Q_SLEEP);
      }
      *(u32x4*)(smem + L_Q + lane * 16) = u32x4{(u32)v[0], (u32)v[1], (u32)v[2], (u32)v[3]};
      lds_arrive(&ctrl[0], lane);
      STAMP(7);
      return;
    }
    if (wave == 0) STAMP(0);
    const size_t kvbase = ((size_t)(bb * HKV + hkv) * SK + KOFF + sp * 352 + wave * 32) * DH;
    const u32x4* kp = (const u32x4*)(p.kc + kvbase + (size_t)m16 * DH + g4 * 8);
    u32x4 kf[8];
#define KV_ISSUE(n) do { \
      if ((n) < 8) { asm volatile("global_load_dwordx4 %0, %1, off nt" : "=v"(kf[(n) & 7]) : "v"(kp + ((n) >> 2) * 16 * (DH / 8) + ((n) & 3) * 4) : "memory"); } \
      else { const int row_ = 32 * wave + 4 * ((n) - 8) + g4; \
             dma16(p.vc + kvbase + (size_t)(4 * ((n) - 8) + g4) * DH + ((m16 ^ vswz(row_)) * 8), smem + L_V + (32 * wave + 4 * ((n) - 8)) * 256); } \
      asm volatile("" ::: "memory"); } while (0)
#pragma unroll
    for (int n = 0; n < KV_WIN; ++n) KV_ISSUE(n);
    __builtin_amdgcn_s_barrier();
#pragma unroll
    for (int n = KV_WIN; n < 16; ++n) {
      asm volatile("s_waitcnt vmcnt(%0)" :: "i"(KV_WIN - 1) : "memory");
      KV_ISSUE(n);
    }
#undef KV_ISSUE
    asm volatile("s_waitcnt vmcnt(8)" : "+v"(kf[0]), "+v"(kf[1]), "+v"(kf[2]), "+v"(kf[3]), "+v"(kf[4]), "+v"(kf[5]), "+v"(kf[6]), "+v"(kf[7]) :: "memory");
    if (wave == 0) STAMP(1);
    lds_spin(&ctrl[0], 1);
    if (wave == 0) STAMP(2);
    bf16x8 qf[4];
#pragma unroll
    for (int t = 0; t < 4; ++t) {
      bf16x8 z = {0, 0, 0, 0, 0, 0, 0, 0};
      if (m16 < 4) z = *(const bf16x8*)(smem + L_Q + (m16 * 128 + t * 32 + g4 * 8) * 2);
      qf[t] = z;
    }
    constexpr float SCALE = 0.08838834764831845f;
    float a[8];
#pragma unroll
    for (int T = 0; T < 2; ++T) {
      f32x4 s = {0.f, 0.f, 0.f, 0.f};
#pragma unroll
      for (int t = 0; t < 4; ++t) s = __builtin_amdgcn_mfma_f32_16x16x32_bf16(__builtin_bit_cast(bf16x8, kf[4 * T + t]), qf[t], s, 0, 0, 0);
#pragma unroll
      for (int i = 0; i < 4; ++i) a[4 * T + i] = bfr(bfr(s[i]) * SCALE);
    }
    float m = a[0];
#pragma unroll
    for (int i = 1; i < 8; ++i) m = fmaxf(m, a[i]);
    m = x32_max(x16_max(m));
    if (lane < 4) ((float*)(smem + L_ML))[wave * 4 + lane] = m;
    lds_arrive(&ctrl[2], lane);
    lds_spin(&ctrl[2], (u32)nkw);
    float M = m;
#pragma unroll
    for (int w = 0; w < 11; ++w)
      if (w < 10 || nkw == 11) M = fmaxf(M, ((const float*)(smem + L_ML))[w * 4 + (m16 & 3)]);
    const float Ms = M * LOG2E;
    float l = 0.f;
    u32x4 pf;
#pragma unroll
    for (int i = 0; i < 4; ++i) {
      const u32 lo = f2bf(__builtin_amdgcn_exp2f(fmaf(a[2 * i], LOG2E, -Ms))), hi = f2bf(__builtin_amdgcn_exp2f(fmaf(a[2 * i + 1], LOG2E, -Ms)));
      pf[i] = lo | (hi << 16);
      l += bf2f(lo) + bf2f(hi);
    }
    l = x32_sum(x16_sum(l));
    *(u32x4*)(smem + L_PB + (wave * 64 + lane) * 16) = pf;
    if (lane < 4) ((float*)(smem + L_SC))[wave * 4 + lane] = l;
    if (wave == 0) STAMP(3);
    vm_wait0();
    lds_arrive(&ctrl[1], lane);
    if (wave >= 8) return;
    lds_spin(&ctrl[1], (u32)nkw);
    if (wave == 0) STAMP(4);
    f32x4 o = {0.f, 0.f, 0.f, 0.f};
    {
      const int q = m16 >> 2, pp = lane & 3;
      const int ch = 2 * wave + (pp >> 1);
      bf16x8 pa[11], vb[11];
#pragma unroll
      for (int w = 0; w < 11; ++w) {
        if (w < 10 || nkw == 11) {
          pa[w] = *(const bf16x8*)(smem + L_PB + (w * 64 + lane) * 16);
          const int r0 = 32 * w + 4 * g4 + q, r1 = r0 + 16;
          const s16x4 lo = __builtin_amdgcn_ds_read_tr16_b64_v4i16((LAS s16x4*)(smem + L_V + r0 * 256 + ((ch ^ vswz(r0)) * 16) + 8 * (pp & 1)));
          const s16x4 hi = __builtin_amdgcn_ds_read_tr16_b64_v4i16((LAS s16x4*)(smem + L_V + r1 * 256 + ((ch ^ vswz(r1)) * 16) + 8 * (pp & 1)));
          vb[w] = bf16x8{lo[0], lo[1], lo[2], lo[3], hi[0], hi[1], hi[2], hi[3]};
        }
      }
      f32x4 o2 = {0.f, 0.f, 0.f, 0.f};
#pragma unroll
      for (int w = 0; w < 10; w += 2) {
        o = __builtin_amdgcn_mfma_f32_16x16x32_bf16(pa[w], vb[w], o, 0, 0, 0);
        o2 = __builtin_amdgcn_mfma_f32_16x16x32_bf16(pa[w + 1], vb[w + 1], o2, 0, 0, 0);
      }
      if (nkw == 11) o = __builtin_amdgcn_mfma_f32_16x16x32_bf16(pa[10], vb[10], o, 0, 0, 0);
      o += o2;
    }
    {
      u64* rec = p.ws + PG_OFF + (((size_t)(hkv * 4) * NB + bb) * NSP + sp) * PREC;
#pragma unroll
      for (int h = 0; h < 4; ++h) {
        const u32 mine = f2bf(o[h]);
        const u32 other = dppu<0xB1>(mine);
        if (lane < 16 && (lane & 1) == 0) put_gran(rec + (size_t)h * NB * NSP * PREC + 8 * wave + (lane >> 1), mine | (other << 16));
      }
      if (wave == 0 && lane < 4) {
        float L = 0.f;
#pragma unroll
        for (int w = 0; w < 11; ++w)
          if (w < 10 || nkw == 11) L += ((const float*)(smem + L_SC))[w * 4 + lane];
        put_gran(rec + (size_t)lane * NB * NSP * PREC + 64, __float_as_uint(M));
        put_gran(rec + (size_t)lane * NB * NSP * PREC + 65, __float_as_uint(L));
      }
    }
    if (wave == 0) STAMP(5);
    return;
  }

  const int j = xg + 8 * (y - 24);
  if (wave == 0) STAMP(8);
  if (tid < 16) ctrl[tid] = 0u;
  const bool has_ffn = j < 56;
  const int fcb = j % 28, fks = j / 28;
  const int rg = j >> 3, cg = j & 7;

  const int ocol = 64 * cg + 8 * rg;
  u32 xr0 = 0, xr1 = 0;
  if (tid < 64) { xr0 = p.x[(tid >> 3) * DM + ocol + (tid & 7)]; xr1 = p.b2[ocol + (tid & 7)]; }
  u32 bias = 0;
  if (tid < 896) bias = *(const u32*)(p.b1 + 224 * rg + 2 * (tid % 112));
  u32x4 xa = {0, 0, 0, 0};
  if (wave < 8) xa = *(const u32x4*)(p.x + wave * DM + lane * 8);
#pragma unroll
  for (int i = 0; i < 4; ++i) {
    const int r0 = 128 * i + 8 * wave;
    dma16(p.wq + (size_t)(r0 + (lane >> 3)) * 4096 + 64 * j + 8 * (lane & 7), smem + L_WQ + r0 * 128);
  }
  if (has_ffn) {
#pragma unroll
    for (int i = 0; i < 2; ++i) {
      const int r0 = 128 * i + 8 * wave;
      dma16(p.w1 + (size_t)(256 * fks + r0 + (lane >> 3)) * FF + 64 * fcb + 8 * (lane & 7), smem + L_W1 + r0 * 128);
    }
  }
  if (wave < 8) {
    float xv[8];
#pragma unroll
    for (int k = 0; k < 4; ++k) { xv[2 * k] = __uint_as_float(xa[k] << 16); xv[2 * k + 1] = __uint_as_float(xa[k] & 0xffff0000u); }
    float s = 0.f;
#pragma unroll
    for (int k = 0; k < 8; ++k) s += xv[k];
    s += dpp<0x121>(s); s += dpp<0x122>(s); s += dpp<0x124>(s); s += dpp<0x128>(s); s = x32_sum(x16_sum(s));
    const float mean = s * (1.f / 512.f);
    float v = 0.f;
#pragma unroll
    for (int k = 0; k < 8; ++k) { const float d = xv[k] - mean; v = fmaf(d, d, v); }
    v += dpp<0x121>(v); v += dpp<0x122>(v); v += dpp<0x124>(v); v += dpp<0x128>(v); v = x32_sum(x16_sum(v));
    const float mb = bfr(mean), vb = bfr(v * (1.f / 512.f));
    const float den = bfr(sqrtf(bfr(vb + 1e-5f)));
    u32x4 o;
#pragma unroll
    for (int k = 0; k < 4; ++k) {
      const float a = bfr(xv[2 * k] - mb) / den, b = bfr(xv[2 * k + 1] - mb) / den;
      o[k] = f2bf(a) | (f2bf(b) << 16);
    }
    *(u32x4*)(smem + L_XN + (wave * XN_STRIDE + lane * 8) * 2) = o;
  }
  vm_wait0();
  lds_wait();
  __builtin_amdgcn_s_barrier();
  if (wave == 0) STAMP(9);
  {
    const int nt = wave & 3, kq = wave >> 2;
    bf16x8 af[4], bq[4], ag[2], bg[2];
#pragma unroll
    for (int k = 0; k < 4; ++k) {
      bf16x8 a = {0, 0, 0, 0, 0, 0, 0, 0};
      if (m16 < 8) a = *(const bf16x8*)(smem + L_XN + (m16 * XN_STRIDE + (4 * kq + k) * 32 + g4 * 8) * 2);
      af[k] = a;
      bq[k] = bfrag_tr(smem + L_WQ, 4 * kq + k, nt, lane);
    }
    if (has_ffn) {
#pragma unroll
      for (int k = 0; k < 2; ++k) {
        bf16x8 a = {0, 0, 0, 0, 0, 0, 0, 0};
        if (m16 < 8) a = *(const bf16x8*)(smem + L_XN + (m16 * XN_STRIDE + 256 * fks + (2 * kq + k) * 32 + g4 * 8) * 2);
        ag[k] = a;
        bg[k] = bfrag_tr(smem + L_W1, 2 * kq + k, nt, lane);
      }
    }
    f32x4 d = {0.f, 0.f, 0.f, 0.f};
#pragma unroll
    for (int k = 0; k < 4; ++k) d = __builtin_amdgcn_mfma_f32_16x16x32_bf16(af[k], bq[k], d, 0, 0, 0);
    if (lane < 32) *(f32x4*)(smem + L_PD + ((kq * 4 + nt) * 32 + lane) * 16) = d;
    if (has_ffn) {
      f32x4 e = {0.f, 0.f, 0.f, 0.f};
#pragma unroll
      for (int k = 0; k < 2; ++k) e = __builtin_amdgcn_mfma_f32_16x16x32_bf16(ag[k], bg[k], e, 0, 0, 0);
      if (lane < 32) *(f32x4*)(smem + L_PD + 8192 + ((kq * 4 + nt) * 32 + lane) * 16) = e;
    }
  }
  if (wave == 0) STAMP(19);
  lds_wait();
  __builtin_amdgcn_s_barrier();
  if (wave == 0) STAMP(20);
  if (wave < 4) {
    f32x4 d = {0.f, 0.f, 0.f, 0.f};
    if (lane < 32) {
#pragma unroll
      for (int kq = 0; kq < 4; ++kq) d += *(const f32x4*)(smem + L_PD + ((kq * 4 + wave) * 32 + lane) * 16);
    }
#pragma unroll
    for (int i = 0; i < 4; ++i) {
      const u32 mine = f2bf(d[i]);
      const u32 other = dppu<0xB1>(mine);
      if (lane < 32 && (lane & 1) == 0)
        put_gran(p.ws + QG_OFF + (size_t)(4 * g4 + i) * 2048 + 32 * j + 8 * wave + (m16 >> 1), mine | (other << 16));
    }
  } else if (wave < 8 && has_ffn) {
    if (lane < 32) {
      f32x4 e = {0.f, 0.f, 0.f, 0.f};
#pragma unroll
      for (int kq = 0; kq < 4; ++kq) e += *(const f32x4*)(smem + L_PD + 8192 + ((kq * 4 + wave - 4) * 32 + lane) * 16);
#pragma unroll
      for (int i = 0; i < 4; ++i)
        put_gran(p.ws + HG_OFF + ((size_t)(4 * g4 + i) * 2 + fks) * FF + 64 * fcb + 16 * (wave - 4) + m16, __float_as_uint(e[i]));
    }
  }
  if (wave < 8) lds_arrive(&ctrl[7], lane);
  if (wave == 0) STAMP(10);
  if (wave >= 14) {
    lds_spin(&ctrl[7], 8);
#pragma unroll
    for (int i = 0; i < 46; ++i) {
      const int r0 = 16 * i + 8 * (wave - 14);
      const int r = r0 + (lane >> 3);
      const u16* src = r0 < 512 ? (p.wo + (size_t)(512 * rg + r) * DM) : (p.w2 + (size_t)(224 * rg + r - 512) * DM);
      dma16(src + 64 * cg + 8 * (lane & 7), smem + L_W3 + r0 * 128);
    }
    vm_wait0();
    lds_arrive(&ctrl[5], lane);
  }
  if (tid < 896) {
    const int b = tid / 112, jj = tid - 112 * b;
    const u64* g = p.ws + HG_OFF + (size_t)b * 2 * FF + 224 * rg + 2 * jj;
    u64 v[4];
    for (u32 it = 0; it < SPIN_CAP; ++it) {
      v[0] = get_gran(g); v[1] = get_gran(g + 1); v[2] = get_gran(g + FF); v[3] = get_gran(g + FF + 1);
      if (tag_ok(v[0]) && tag_ok(v[1]) && tag_ok(v[2]) && tag_ok(v[3])) break;
      __builtin_amdgcn_s_sleep(4);
    }
    float h0 = __uint_as_float((u32)v[0]) + __uint_as_float((u32)v[2]);
    float h1 = __uint_as_float((u32)v[1]) + __uint_as_float((u32)v[3]);
    h0 = gelu_tanh(bfr(bfr(h0) + bf2f(bias & 0xffffu)));
    h1 = gelu_tanh(bfr(bfr(h1) + bf2f(bias >> 16)));
    ((u32*)(smem + L_A3))[b * (K3_STRIDE / 2) + 256 + jj] = f2bf(h0) | (f2bf(h1) << 16);
  }
  lds_arrive(&ctrl[6], lane);
  lds_spin(&ctrl[6], 16);
  lds_spin(&ctrl[5], 2);
  f32x4 dacc = {0.f, 0.f, 0.f, 0.f};
  bf16x8 b3[16];
  if (wave < 4) {
#pragma unroll
    for (int ks = 16; ks < 23; ++ks) {
      bf16x8 a = {0, 0, 0, 0, 0, 0, 0, 0};
      if (m16 < 8) a = *(const bf16x8*)(smem + L_A3 + (m16 * K3_STRIDE + ks * 32 + g4 * 8) * 2);
      dacc = __builtin_amdgcn_mfma_f32_16x16x32_bf16(a, bfrag_tr(smem + L_W3, ks, wave, lane), dacc, 0, 0, 0);
    }
#pragma unroll
    for (int k = 0; k < 16; ++k) b3[k] = bfrag_tr(smem + L_W3, k, wave, lane);
  }
  if (wave == 0) STAMP(11);
  {
    const int hh = tid >> 8, b = (tid >> 5) & 7, jj = tid & 31;
    const size_t base = PG_OFF + ((size_t)(4 * rg + hh) * NB + b) * NSP * PREC;
    const __amdgpu_buffer_rsrc_t rs = __builtin_amdgcn_make_buffer_rsrc((void*)p.ws, 0, (int)WS_BYTES, 0x00020000);
    u32x4 ov[NSP], ml[NSP];
    for (u32 it = 0; it < SPIN_CAP; ++it) {
      bool ok = true;
#pragma unroll
      for (int s = 0; s < NSP; ++s) {
        ov[s] = get_gran2(rs, base + s * PREC + 2 * jj);
        ml[s] = get_gran2(rs, base + s * PREC + 64);
      }
#pragma unroll
      for (int s = 0; s < NSP; ++s) ok = ok && ov[s][1] == TAG && ov[s][3] == TAG && ml[s][1] == TAG && ml[s][3] == TAG;
      if (__all(ok)) break;
      __builtin_amdgcn_s_sleep(PG_SLEEP);
    }
    float M = -INFINITY;
#pragma unroll
    for (int s = 0; s < NSP; ++s) M = fmaxf(M, __uint_as_float(ml[s][0]));
    float L = 0.f, o[4] = {0.f, 0.f, 0.f, 0.f};
#pragma unroll
    for (int s = 0; s < NSP; ++s) {
      const float wt = __builtin_amdgcn_exp2f((__uint_as_float(ml[s][0]) - M) * LOG2E);
      L = fmaf(wt, __uint_as_float(ml[s][2]), L);
      o[0] = fmaf(wt, bf2f(ov[s][0] & 0xffffu), o[0]);
      o[1] = fmaf(wt, bf2f(ov[s][0] >> 16), o[1]);
      o[2] = fmaf(wt, bf2f(ov[s][2] & 0xffffu), o[2]);
      o[3] = fmaf(wt, bf2f(ov[s][2] >> 16), o[3]);
    }
    const float inv = 1.f / L;
    u32* a3 = (u32*)(smem + L_A3) + b * (K3_STRIDE / 2) + 64 * hh + 2 * jj;
    a3[0] = f2bf(o[0] * inv) | (f2bf(o[1] * inv) << 16);
    a3[1] = f2bf(o[2] * inv) | (f2bf(o[3] * inv) << 16);
  }
  if (wave == 0) STAMP(12);
  lds_wait();
  __builtin_amdgcn_s_barrier();
  if (wave == 0) STAMP(13);
  if (wave < 4) {
    f32x4 d2 = {0.f, 0.f, 0.f, 0.f};
#pragma unroll
    for (int hf = 0; hf < 2; ++hf) {
      bf16x8 af[8];
#pragma unroll
      for (int k = 0; k < 8; ++k) {
        bf16x8 a = {0, 0, 0, 0, 0, 0, 0, 0};
        if (m16 < 8) a = *(const bf16x8*)(smem + L_A3 + (m16 * K3_STRIDE + (8 * hf + k) * 32 + g4 * 8) * 2);
        af[k] = a;
      }
#pragma unroll
      for (int k = 0; k < 8; k += 2) {
        dacc = __builtin_amdgcn_mfma_f32_16x16x32_bf16(af[k], b3[8 * hf + k], dacc, 0, 0, 0);
        d2 = __builtin_amdgcn_mfma_f32_16x16x32_bf16(af[k + 1], b3[8 * hf + k + 1], d2, 0, 0, 0);
      }
      __builtin_amdgcn_sched_barrier(0);
    }
    dacc += d2;
    if (lane < 32) {
      const int col = 16 * wave + m16;
      u64* og = p.ws + OG_OFF + ((((size_t)cg * 8 + (col >> 3)) * 8 + rg) * 8) * 8 + (col & 7);
#pragma unroll
      for (int i = 0; i < 4; ++i) put_gran(og + (4 * g4 + i) * 8, __float_as_uint(dacc[i]));
    }
  }
  if (wave == 0) STAMP(14);
  if (wave == 0) {
    const u64* base = p.ws + OG_OFF + (((size_t)cg * 8 + rg) * 8) * 64 + lane;
    u64 a[8];
    for (u32 it = 0; it < SPIN_CAP; ++it) {
      bool ok = true;
#pragma unroll
      for (int w = 0; w < 8; ++w) a[w] = get_gran(base + w * 64);
#pragma unroll
      for (int w = 0; w < 8; ++w) ok = ok && tag_ok(a[w]);
      if (__all(ok)) break;
      __builtin_amdgcn_s_sleep(1);
    }
    STAMP(15);
    float r = bf2f(xr0) + bf2f(xr1);
#pragma unroll
    for (int w = 0; w < 8; ++w) r += __uint_as_float((u32)a[w]);
    p.out[(lane >> 3) * DM + ocol + (lane & 7)] = (u16)f2bf(r);
    STAMP(16);
  }
}

}

extern "C" void kernel_launch(void* const* d_in, const int* in_sizes, int n_in,
                              void* d_out, int out_size, void* d_ws, size_t ws_size,
                              hipStream_t stream) {
  Params p;
  p.x = (const u16*)d_in[0];
  p.kc = (const u16*)d_in[1];
  p.vc = (const u16*)d_in[2];
  p.wq = (const u16*)d_in[3];
  p.wo = (const u16*)d_in[4];
  p.w1 = (const u16*)d_in[6];
  p.b1 = (const u16*)d_in[7];
  p.w2 = (const u16*)d_in[8];
  p.b2 = (const u16*)d_in[9];
  p.out = (u16*)d_out;
  p.ws = (u64*)d_ws;
  (void)hipMemsetAsync(d_ws, 0, WS_BYTES, stream);
  hipLaunchKernelGGL(fused_decode, dim3(NWG), dim3(NTHR), 0, stream, p);
}
```

```cpp
#include <hip/hip_runtime.h>
#include <hip/hip_bf16.h>
#include <stdint.h>

typedef unsigned int u32;
typedef unsigned long long u64;
typedef unsigned short u16;
typedef u32 u32x4 __attribute__((ext_vector_type(4)));
typedef float f32x4 __attribute__((ext_vector_type(4)));
typedef short bf16x8 __attribute__((ext_vector_type(8)));
typedef short s16x4 __attribute__((ext_vector_type(4)));

#define GAS __attribute__((address_space(1)))
#define LAS __attribute__((address_space(3)))
#define RLX_AGENT __ATOMIC_RELAXED, __HIP_MEMORY_SCOPE_AGENT

namespace {

constexpr int NB = 8, DM = 512, HQ = 32, HKV = 8, SK = 4096, DH = 128, WINDOW = 1024, FF = 1792;
constexpr int KOFF = SK - WINDOW;
constexpr int NWG = 256, NTHR = 1024;
constexpr u32 SPIN_CAP = 1u << 22;
constexpr float LOG2E = 1.4426950408889634f;

constexpr int NSP = 3;
constexpr int PREC = 66;
constexpr size_t QG_OFF = 0, QG_N = (size_t)NB * 2048;
constexpr size_t HG_OFF = QG_OFF + QG_N, HG_N = (size_t)NB * 2 * FF;
constexpr size_t PG_OFF = HG_OFF + HG_N, PG_N = (size_t)HQ * NB * NSP * PREC;
constexpr size_t OG_OFF = PG_OFF + PG_N, OG_N = (size_t)8 * 8 * 8 * 8 * 8;
constexpr size_t EP_OFF = OG_OFF + OG_N;
constexpr size_t WS_GRAN = EP_OFF + 16;
constexpr size_t WS_BYTES = WS_GRAN * 8;
static_assert(WS_BYTES % 16 == 0, "memset block must be a multiple of 16 bytes");

constexpr int L_CTRL = 0;
constexpr int L_Q = 64;
constexpr int L_ML = L_Q + 1024;
constexpr int L_SC = L_ML + 512;
constexpr int L_PB = L_SC + 2048;
constexpr int L_V = L_PB + 11 * 1024;
constexpr int LA_END = L_V + 352 * 256;
constexpr int XN_STRIDE = 520;
constexpr int K3 = 736, K3_STRIDE = 744;
constexpr int L_XN = 64;
constexpr int L_A3 = L_XN + NB * XN_STRIDE * 2;
constexpr int L_RED = L_A3 + NB * K3_STRIDE * 2;
constexpr int L_WQ = L_RED + 2048;
constexpr int L_W1 = L_WQ + 65536;
constexpr int L_W3 = L_WQ;
constexpr int L_PD = L_W1 + 32768;
constexpr int LB_END = L_PD + 16384;
static_assert(L_W3 + K3 * 128 <= LB_END, "W3 overlay");
constexpr int LDS_BYTES = LA_END > LB_END ? LA_END : LB_END;
static_assert(LDS_BYTES > 80 * 1024 && LDS_BYTES <= 160 * 1024, "one workgroup per CU");
static_assert(L_WQ % 16 == 0 && L_A3 % 16 == 0 && L_V % 16 == 0 && L_PB % 16 == 0 && L_RED % 16 == 0, "align");

struct Params {
  const u16* x; const u16* kc; const u16* vc; const u16* wq; const u16* wo;
  const u16* w1; const u16* b1; const u16* w2; const u16* b2;
  u16* out; u64* ws;
};

__device__ __forceinline__ float bf2f(u32 h) { return __uint_as_float(h << 16); }
__device__ __forceinline__ u32 f2bf(float f) {
  __bf16 h = (__bf16)f;
  return (u32)__builtin_bit_cast(u16, h);
}
__device__ __forceinline__ float bfr(float f) { return bf2f(f2bf(f)); }

template <int CTRL> __device__ __forceinline__ float dpp(float x) {
  return __builtin_bit_cast(float, __builtin_amdgcn_mov_dpp(__builtin_bit_cast(int, x), CTRL, 0xf, 0xf, true));
}
template <int CTRL> __device__ __forceinline__ u32 dppu(u32 x) {
  return (u32)__builtin_amdgcn_mov_dpp((int)x, CTRL, 0xf, 0xf, true);
}
__device__ __forceinline__ float x16_max(float x) {
  auto s = __builtin_amdgcn_permlane16_swap(__float_as_uint(x), __float_as_uint(x), false, false);
  return fmaxf(__uint_as_float(s[0]), __uint_as_float(s[1]));
}
__device__ __forceinline__ float x32_max(float x) {
  auto t = __builtin_amdgcn_permlane32_swap(__float_as_uint(x), __float_as_uint(x), false, false);
  return fmaxf(__uint_as_float(t[0]), __uint_as_float(t[1]));
}
__device__ __forceinline__ float x16_sum(float x) {
  auto s = __builtin_amdgcn_permlane16_swap(__float_as_uint(x), __float_as_uint(x), false, false);
  return __uint_as_float(s[0]) + __uint_as_float(s[1]);
}
__device__ __forceinline__ float x32_sum(float x) {
  auto t = __builtin_amdgcn_permlane32_swap(__float_as_uint(x), __float_as_uint(x), false, false);
  return __uint_as_float(t[0]) + __uint_as_float(t[1]);
}

__device__ __forceinline__ void put_gran_t(u64* g, u32 v, u32 tag) {
  __hip_atomic_store((GAS u64*)g, ((u64)tag << 32) | (u64)v, RLX_AGENT);
}
__device__ __forceinline__ u64 get_gran(const u64* g) {
  return __hip_atomic_load((GAS u64*)g, RLX_AGENT);
}
#define tag_ok(v) ((u32)((v) >> 32) == TAG)
#define put_gran(g, v) put_gran_t((g), (v), TAG)
__device__ __forceinline__ u32x4 get_gran2(__amdgpu_buffer_rsrc_t rs, size_t gidx) {
  return __builtin_amdgcn_raw_buffer_load_b128(rs, (int)(gidx * 8), 0, 16);
}

__device__ __forceinline__ void lds_wait() { asm volatile("s_waitcnt lgkmcnt(0)" ::: "memory"); }
__device__ __forceinline__ void vm_wait0() { asm volatile("s_waitcnt vmcnt(0)" ::: "memory"); }

typedef volatile LAS u32 lctr;
__device__ __forceinline__ void lds_add1(lctr* c) {
  __hip_atomic_fetch_add((LAS u32*)c, 1u, __ATOMIC_RELAXED, __HIP_MEMORY_SCOPE_WORKGROUP);
}
__device__ __forceinline__ void lds_arrive(lctr* c, int lane) {
  lds_wait();
  if (lane == 0) lds_add1(c);
  asm volatile("" ::: "memory");
}
__device__ __forceinline__ void lds_spin(lctr* c, u32 target) {
  for (u32 it = 0; it < SPIN_CAP; ++it) {
    if (*c >= target) break;
    __builtin_amdgcn_s_sleep(1);
  }
  asm volatile("" ::: "memory");
}

__device__ __forceinline__ float gelu_tanh(float v) {
  const float u = 0.7978845608028654f * (v + 0.044715f * v * v * v);
  return 0.5f * v * (1.f + tanhf(u));
}

__device__ __forceinline__ void dma16(const void* gsrc, unsigned char* lds_base) {
  __builtin_amdgcn_global_load_lds(gsrc, (LAS void*)lds_base, 16, 0, 2);
}
__device__ __forceinline__ bf16x8 bfrag_tr(const unsigned char* tile, int ks, int nt, int lane) {
  const int g = lane >> 4, q = (lane & 15) >> 2, pp = lane & 3;
  const unsigned char* a = tile + (32 * ks + 8 * g + q) * 128 + (16 * nt + 4 * pp) * 2;
  const s16x4 lo = __builtin_amdgcn_ds_read_tr16_b64_v4i16((LAS s16x4*)a);
  const s16x4 hi = __builtin_amdgcn_ds_read_tr16_b64_v4i16((LAS s16x4*)(a + 4 * 128));
  return bf16x8{lo[0], lo[1], lo[2], lo[3], hi[0], hi[1], hi[2], hi[3]};
}
__device__ __forceinline__ int vswz(int row) { return ((row & 3) << 2) | ((row >> 2) & 3); }

#define KV_WIN 5
#define Q_SLEEP 8
#define PG_SLEEP 4
#define STAMP(k) do { } while (0)

__global__ void __launch_bounds__(NTHR, 4) fused_decode(Params p) {
  __shared__ __attribute__((aligned(16))) unsigned char smem[LDS_BYTES];
  lctr* ctrl = (lctr*)(smem + L_CTRL);

  const int tid = threadIdx.x, lane = tid & 63;
  const int wave = __builtin_amdgcn_readfirstlane(tid >> 6);
  const int bid = blockIdx.x, xg = bid & 7, y = bid >> 3;
  const int g4 = lane >> 4, m16 = lane & 15;

  if (y < 24) {
    const int hkv = xg, bb = y / 3, sp = y - 3 * bb;
    const int nkw = sp == 2 ? 10 : 11;
    if (wave > 11 || (wave == 10 && nkw == 10)) return;
    if (wave == 11) {
      const u32 TAG = __hip_atomic_load((GAS u32*)(p.ws + EP_OFF), RLX_AGENT) + 1u;
      if (lane < 16) ctrl[lane] = 0u;
      lds_wait();
      __builtin_amdgcn_s_barrier();
      STAMP(6);
      const u64* g = p.ws + QG_OFF + (size_t)bb * 2048 + hkv * 256 + 4 * lane;
      u64 v[4];
      for (u32 it = 0; it < SPIN_CAP; ++it) {
#pragma unroll
        for (int k = 0; k < 4; ++k) v[k] = get_gran(g + k);
        if (__all(tag_ok(v[0]) && tag_ok(v[1]) && tag_ok(v[2]) && tag_ok(v[3]))) break;
        __builtin_amdgcn_s_sleep(Q_SLEEP);
      }
      *(u32x4*)(smem + L_Q + lane * 16) = u32x4{(u32)v[0], (u32)v[1], (u32)v[2], (u32)v[3]};
      if (lane == 0) ctrl[8] = TAG;
      lds_arrive(&ctrl[0], lane);
      STAMP(7);
      return;
    }
    if (wave == 0) STAMP(0);
    const size_t kvbase = ((size_t)(bb * HKV + hkv) * SK + KOFF + sp * 352 + wave * 32) * DH;
    const u32x4* kp = (const u32x4*)(p.kc + kvbase + (size_t)m16 * DH + g4 * 8);
    u32x4 kf[8];
#define KV_ISSUE(n) do { \
      if ((n) < 8) { asm volatile("global_load_dwordx4 %0, %1, off nt" : "=v"(kf[(n) & 7]) : "v"(kp + ((n) >> 2) * 16 * (DH / 8) + ((n) & 3) * 4) : "memory"); } \
      else { const int row_ = 32 * wave + 4 * ((n) - 8) + g4; \
             dma16(p.vc + kvbase + (size_t)(4 * ((n) - 8) + g4) * DH + ((m16 ^ vswz(row_)) * 8), smem + L_V + (32 * wave + 4 * ((n) - 8)) * 256); } \
      asm volatile("" ::: "memory"); } while (0)
#pragma unroll
    for (int n = 0; n < KV_WIN; ++n) KV_ISSUE(n);
    __builtin_amdgcn_s_barrier();
#pragma unroll
    for (int n = KV_WIN; n < 16; ++n) {
      asm volatile("s_waitcnt vmcnt(%0)" :: "i"(KV_WIN - 1) : "memory");
      KV_ISSUE(n);
    }
#undef KV_ISSUE
    asm volatile("s_waitcnt vmcnt(8)" : "+v"(kf[0]), "+v"(kf[1]), "+v"(kf[2]), "+v"(kf[3]), "+v"(kf[4]), "+v"(kf[5]), "+v"(kf[6]), "+v"(kf[7]) :: "memory");
    if (wave == 0) STAMP(1);
    lds_spin(&ctrl[0], 1);
    const u32 TAG = ctrl[8];
    if (wave == 0) STAMP(2);
    bf16x8 qf[4];
#pragma unroll
    for (int t = 0; t < 4; ++t) {
      bf16x8 z = {0, 0, 0, 0, 0, 0, 0, 0};
      if (m16 < 4) z = *(const bf16x8*)(smem + L_Q + (m16 * 128 + t * 32 + g4 * 8) * 2);
      qf[t] = z;
    }
    constexpr float SCALE = 0.08838834764831845f;
    float a[8];
#pragma unroll
    for (int T = 0; T < 2; ++T) {
      f32x4 s = {0.f, 0.f, 0.f, 0.f};
#pragma unroll
      for (int t = 0; t < 4; ++t) s = __builtin_amdgcn_mfma_f32_16x16x32_bf16(__builtin_bit_cast(bf16x8, kf[4 * T + t]), qf[t], s, 0, 0, 0);
#pragma unroll
      for (int i = 0; i < 4; ++i) a[4 * T + i] = bfr(bfr(s[i]) * SCALE);
    }
    float m = a[0];
#pragma unroll
    for (int i = 1; i < 8; ++i) m = fmaxf(m, a[i]);
    m = x32_max(x16_max(m));
    if (lane < 4) ((float*)(smem + L_ML))[wave * 4 + lane] = m;
    lds_arrive(&ctrl[2], lane);
    lds_spin(&ctrl[2], (u32)nkw);
    float M = m;
#pragma unroll
    for (int w = 0; w < 11; ++w)
      if (w < 10 || nkw == 11) M = fmaxf(M, ((const float*)(smem + L_ML))[w * 4 + (m16 & 3)]);
    const float Ms = M * LOG2E;
    float l = 0.f;
    u32x4 pf;
#pragma unroll
    for (int i = 0; i < 4; ++i) {
      const u32 lo = f2bf(__builtin_amdgcn_exp2f(fmaf(a[2 * i], LOG2E, -Ms))), hi = f2bf(__builtin_amdgcn_exp2f(fmaf(a[2 * i + 1], LOG2E, -Ms)));
      pf[i] = lo | (hi << 16);
      l += bf2f(lo) + bf2f(hi);
    }
    l = x32_sum(x16_sum(l));
    *(u32x4*)(smem + L_PB + (wave * 64 + lane) * 16) = pf;
    if (lane < 4) ((float*)(smem + L_SC))[wave * 4 + lane] = l;
    if (wave == 0) STAMP(3);
    vm_wait0();
    lds_arrive(&ctrl[1], lane);
    if (wave >= 8) return;
    lds_spin(&ctrl[1], (u32)nkw);
    if (wave == 0) STAMP(4);
    f32x4 o = {0.f, 0.f, 0.f, 0.f};
    {
      const int q = m16 >> 2, pp = lane & 3;
      const int ch = 2 * wave + (pp >> 1);
      bf16x8 pa[11], vb[11];
#pragma unroll
      for (int w = 0; w < 11; ++w) {
        if (w < 10 || nkw == 11) {
          pa[w] = *(const bf16x8*)(smem + L_PB + (w * 64 + lane) * 16);
          const int r0 = 32 * w + 4 * g4 + q, r1 = r0 + 16;
          const s16x4 lo = __builtin_amdgcn_ds_read_tr16_b64_v4i16((LAS s16x4*)(smem + L_V + r0 * 256 + ((ch ^ vswz(r0)) * 16) + 8 * (pp & 1)));
          const s16x4 hi = __builtin_amdgcn_ds_read_tr16_b64_v4i16((LAS s16x4*)(smem + L_V + r1 * 256 + ((ch ^ vswz(r1)) * 16) + 8 * (pp & 1)));
          vb[w] = bf16x8{lo[0], lo[1], lo[2], lo[3], hi[0], hi[1], hi[2], hi[3]};
        }
      }
      f32x4 o2 = {0.f, 0.f, 0.f, 0.f};
#pragma unroll
      for (int w = 0; w < 10; w += 2) {
        o = __builtin_amdgcn_mfma_f32_16x16x32_bf16(pa[w], vb[w], o, 0, 0, 0);
        o2 = __builtin_amdgcn_mfma_f32_16x16x32_bf16(pa[w + 1], vb[w + 1], o2, 0, 0, 0);
      }
      if (nkw == 11) o = __builtin_amdgcn_mfma_f32_16x16x32_bf16(pa[10], vb[10], o, 0, 0, 0);
      o += o2;
    }
    {
      u64* rec = p.ws + PG_OFF + (((size_t)(hkv * 4) * NB + bb) * NSP + sp) * PREC;
#pragma unroll
      for (int h = 0; h < 4; ++h) {
        const u32 mine = f2bf(o[h]);
        const u32 other = dppu<0xB1>(mine);
        if (lane < 16 && (lane & 1) == 0) put_gran(rec + (size_t)h * NB * NSP * PREC + 8 * wave + (lane >> 1), mine | (other << 16));
      }
      if (wave == 0 && lane < 4) {
        float L = 0.f;
#pragma unroll
        for (int w = 0; w < 11; ++w)
          if (w < 10 || nkw == 11) L += ((const float*)(smem + L_SC))[w * 4 + lane];
        put_gran(rec + (size_t)lane * NB * NSP * PREC + 64, __float_as_uint(M));
        put_gran(rec + (size_t)lane * NB * NSP * PREC + 65, __float_as_uint(L));
      }
    }
    if (wave == 0) STAMP(5);
    return;
  }

  const int j = xg + 8 * (y - 24);
  const u32 TAG = __hip_atomic_load((GAS u32*)(p.ws + EP_OFF), RLX_AGENT) + 1u;
  if (wave == 0) STAMP(8);
  if (tid < 16) ctrl[tid] = 0u;
  const bool has_ffn = j < 56;
  const int fcb = j % 28, fks = j / 28;
  const int rg = j >> 3, cg = j & 7;

  const int ocol = 64 * cg + 8 * rg;
  u32 xr0 = 0, xr1 = 0;
  if (tid < 64) { xr0 = p.x[(tid >> 3) * DM + ocol + (tid & 7)]; xr1 = p.b2[ocol + (tid & 7)]; }
  u32 bias = 0;
  if (tid < 896) bias = *(const u32*)(p.b1 + 224 * rg + 2 * (tid % 112));
  u32x4 xa = {0, 0, 0, 0};
  if (wave < 8) xa = *(const u32x4*)(p.x + wave * DM + lane * 8);
#pragma unroll
  for (int i = 0; i < 4; ++i) {
    const int r0 = 128 * i + 8 * wave;
    dma16(p.wq + (size_t)(r0 + (lane >> 3)) * 4096 + 64 * j + 8 * (lane & 7), smem + L_WQ + r0 * 128);
  }
  if (has_ffn) {
#pragma unroll
    for (int i = 0; i < 2; ++i) {
      const int r0 = 128 * i + 8 * wave;
      dma16(p.w1 + (size_t)(256 * fks + r0 + (lane >> 3)) * FF + 64 * fcb + 8 * (lane & 7), smem + L_W1 + r0 * 128);
    }
  }
  if (wave < 8) {
    float xv[8];
#pragma unroll
    for (int k = 0; k < 4; ++k) { xv[2 * k] = __uint_as_float(xa[k] << 16); xv[2 * k + 1] = __uint_as_float(xa[k] & 0xffff0000u); }
    float s = 0.f;
#pragma unroll
    for (int k = 0; k < 8; ++k) s += xv[k];
    s += dpp<0x121>(s); s += dpp<0x122>(s); s += dpp<0x124>(s); s += dpp<0x128>(s); s = x32_sum(x16_sum(s));
    const float mean = s * (1.f / 512.f);
    float v = 0.f;
#pragma unroll
    for (int k = 0; k < 8; ++k) { const float d = xv[k] - mean; v = fmaf(d, d, v); }
    v += dpp<0x121>(v); v += dpp<0x122>(v); v += dpp<0x124>(v); v += dpp<0x128>(v); v = x32_sum(x16_sum(v));
    const float mb = bfr(mean), vb = bfr(v * (1.f / 512.f));
    const float den = bfr(sqrtf(bfr(vb + 1e-5f)));
    u32x4 o;
#pragma unroll
    for (int k = 0; k < 4; ++k) {
      const float a = bfr(xv[2 * k] - mb) / den, b = bfr(xv[2 * k + 1] - mb) / den;
      o[k] = f2bf(a) | (f2bf(b) << 16);
    }
    *(u32x4*)(smem + L_XN + (wave * XN_STRIDE + lane * 8) * 2) = o;
  }
  vm_wait0();
  lds_wait();
  __builtin_amdgcn_s_barrier();
  if (wave == 0) STAMP(9);
  {
    const int nt = wave & 3, kq = wave >> 2;
    bf16x8 af[4], bq[4], ag[2], bg[2];
#pragma unroll
    for (int k = 0; k < 4; ++k) {
      bf16x8 a = {0, 0, 0, 0, 0, 0, 0, 0};
      if (m16 < 8) a = *(const bf16x8*)(smem + L_XN + (m16 * XN_STRIDE + (4 * kq + k) * 32 + g4 * 8) * 2);
      af[k] = a;
      bq[k] = bfrag_tr(smem + L_WQ, 4 * kq + k, nt, lane);
    }
    if (has_ffn) {
#pragma unroll
      for (int k = 0; k < 2; ++k) {
        bf16x8 a = {0, 0, 0, 0, 0, 0, 0, 0};
        if (m16 < 8) a = *(const bf16x8*)(smem + L_XN + (m16 * XN_STRIDE + 256 * fks + (2 * kq + k) * 32 + g4 * 8) * 2);
        ag[k] = a;
        bg[k] = bfrag_tr(smem + L_W1, 2 * kq + k, nt, lane);
      }
    }
    f32x4 d = {0.f, 0.f, 0.f, 0.f};
#pragma unroll
    for (int k = 0; k < 4; ++k) d = __builtin_amdgcn_mfma_f32_16x16x32_bf16(af[k], bq[k], d, 0, 0, 0);
    if (lane < 32) *(f32x4*)(smem + L_PD + ((kq * 4 + nt) * 32 + lane) * 16) = d;
    if (has_ffn) {
      f32x4 e = {0.f, 0.f, 0.f, 0.f};
#pragma unroll
      for (int k = 0; k < 2; ++k) e = __builtin_amdgcn_mfma_f32_16x16x32_bf16(ag[k], bg[k], e, 0, 0, 0);
      if (lane < 32) *(f32x4*)(smem + L_PD + 8192 + ((kq * 4 + nt) * 32 + lane) * 16) = e;
    }
  }
  if (wave == 0) STAMP(19);
  lds_wait();
  __builtin_amdgcn_s_barrier();
  if (wave == 0) STAMP(20);
  if (wave < 4) {
    f32x4 d = {0.f, 0.f, 0.f, 0.f};
    if (lane < 32) {
#pragma unroll
      for (int kq = 0; kq < 4; ++kq) d += *(const f32x4*)(smem + L_PD + ((kq * 4 + wave) * 32 + lane) * 16);
    }
#pragma unroll
    for (int i = 0; i < 4; ++i) {
      const u32 mine = f2bf(d[i]);
      const u32 other = dppu<0xB1>(mine);
      if (lane < 32 && (lane & 1) == 0)
        put_gran(p.ws + QG_OFF + (size_t)(4 * g4 + i) * 2048 + 32 * j + 8 * wave + (m16 >> 1), mine | (other << 16));
    }
  } else if (wave < 8 && has_ffn) {
    if (lane < 32) {
      f32x4 e = {0.f, 0.f, 0.f, 0.f};
#pragma unroll
      for (int kq = 0; kq < 4; ++kq) e += *(const f32x4*)(smem + L_PD + 8192 + ((kq * 4 + wave - 4) * 32 + lane) * 16);
#pragma unroll
      for (int i = 0; i < 4; ++i)
        put_gran(p.ws + HG_OFF + ((size_t)(4 * g4 + i) * 2 + fks) * FF + 64 * fcb + 16 * (wave - 4) + m16, __float_as_uint(e[i]));
    }
  }
  if (wave < 8) lds_arrive(&ctrl[7], lane);
  if (wave == 0) STAMP(10);
  if (wave >= 14) {
    lds_spin(&ctrl[7], 8);
#pragma unroll
    for (int i = 0; i < 46; ++i) {
      const int r0 = 16 * i + 8 * (wave - 14);
      const int r = r0 + (lane >> 3);
      const u16* src = r0 < 512 ? (p.wo + (size_t)(512 * rg + r) * DM) : (p.w2 + (size_t)(224 * rg + r - 512) * DM);
      dma16(src + 64 * cg + 8 * (lane & 7), smem + L_W3 + r0 * 128);
    }
    vm_wait0();
    lds_arrive(&ctrl[5], lane);
  }
  if (tid < 896) {
    const int b = tid / 112, jj = tid - 112 * b;
    const u64* g = p.ws + HG_OFF + (size_t)b * 2 * FF + 224 * rg + 2 * jj;
    u64 v[4];
    for (u32 it = 0; it < SPIN_CAP; ++it) {
      v[0] = get_gran(g); v[1] = get_gran(g + 1); v[2] = get_gran(g + FF); v[3] = get_gran(g + FF + 1);
      if (tag_ok(v[0]) && tag_ok(v[1]) && tag_ok(v[2]) && tag_ok(v[3])) break;
      __builtin_amdgcn_s_sleep(4);
    }
    float h0 = __uint_as_float((u32)v[0]) + __uint_as_float((u32)v[2]);
    float h1 = __uint_as_float((u32)v[1]) + __uint_as_float((u32)v[3]);
    h0 = gelu_tanh(bfr(bfr(h0) + bf2f(bias & 0xffffu)));
    h1 = gelu_tanh(bfr(bfr(h1) + bf2f(bias >> 16)));
    ((u32*)(smem + L_A3))[b * (K3_STRIDE / 2) + 256 + jj] = f2bf(h0) | (f2bf(h1) << 16);
  }
  lds_arrive(&ctrl[6], lane);
  lds_spin(&ctrl[6], 16);
  lds_spin(&ctrl[5], 2);
  f32x4 dacc = {0.f, 0.f, 0.f, 0.f};
  bf16x8 b3[16];
  if (wave < 4) {
#pragma unroll
    for (int ks = 16; ks < 23; ++ks) {
      bf16x8 a = {0, 0, 0, 0, 0, 0, 0, 0};
      if (m16 < 8) a = *(const bf16x8*)(smem + L_A3 + (m16 * K3_STRIDE + ks * 32 + g4 * 8) * 2);
      dacc = __builtin_amdgcn_mfma_f32_16x16x32_bf16(a, bfrag_tr(smem + L_W3, ks, wave, lane), dacc, 0, 0, 0);
    }
#pragma unroll
    for (int k = 0; k < 16; ++k) b3[k] = bfrag_tr(smem + L_W3, k, wave, lane);
  }
  if (wave == 0) STAMP(11);
  {
    const int hh = tid >> 8, b = (tid >> 5) & 7, jj = tid & 31;
    const size_t base = PG_OFF + ((size_t)(4 * rg + hh) * NB + b) * NSP * PREC;
    const __amdgpu_buffer_rsrc_t rs = __builtin_amdgcn_make_buffer_rsrc((void*)p.ws, 0, (int)WS_BYTES, 0x00020000);
    u32x4 ov[NSP], ml[NSP];
    for (u32 it = 0; it < SPIN_CAP; ++it) {
      bool ok = true;
#pragma unroll
      for (int s = 0; s < NSP; ++s) {
        ov[s] = get_gran2(rs, base + s * PREC + 2 * jj);
        ml[s] = get_gran2(rs, base + s * PREC + 64);
      }
#pragma unroll
      for (int s = 0; s < NSP; ++s) ok = ok && ov[s][1] == TAG && ov[s][3] == TAG && ml[s][1] == TAG && ml[s][3] == TAG;
      if (__all(ok)) break;
      __builtin_amdgcn_s_sleep(PG_SLEEP);
    }
    float M = -INFINITY;
#pragma unroll
    for (int s = 0; s < NSP; ++s) M = fmaxf(M, __uint_as_float(ml[s][0]));
    float L = 0.f, o[4] = {0.f, 0.f, 0.f, 0.f};
#pragma unroll
    for (int s = 0; s < NSP; ++s) {
      const float wt = __builtin_amdgcn_exp2f((__uint_as_float(ml[s][0]) - M) * LOG2E);
      L = fmaf(wt, __uint_as_float(ml[s][2]), L);
      o[0] = fmaf(wt, bf2f(ov[s][0] & 0xffffu), o[0]);
      o[1] = fmaf(wt, bf2f(ov[s][0] >> 16), o[1]);
      o[2] = fmaf(wt, bf2f(ov[s][2] & 0xffffu), o[2]);
      o[3] = fmaf(wt, bf2f(ov[s][2] >> 16), o[3]);
    }
    const float inv = 1.f / L;
    u32* a3 = (u32*)(smem + L_A3) + b * (K3_STRIDE / 2) + 64 * hh + 2 * jj;
    a3[0] = f2bf(o[0] * inv) | (f2bf(o[1] * inv) << 16);
    a3[1] = f2bf(o[2] * inv) | (f2bf(o[3] * inv) << 16);
  }
  if (wave == 0) STAMP(12);
  lds_wait();
  __builtin_amdgcn_s_barrier();
  if (wave == 0) STAMP(13);
  if (wave < 4) {
    f32x4 d2 = {0.f, 0.f, 0.f, 0.f};
#pragma unroll
    for (int hf = 0; hf < 2; ++hf) {
      bf16x8 af[8];
#pragma unroll
      for (int k = 0; k < 8; ++k) {
        bf16x8 a = {0, 0, 0, 0, 0, 0, 0, 0};
        if (m16 < 8) a = *(const bf16x8*)(smem + L_A3 + (m16 * K3_STRIDE + (8 * hf + k) * 32 + g4 * 8) * 2);
        af[k] = a;
      }
#pragma unroll
      for (int k = 0; k < 8; k += 2) {
        dacc = __builtin_amdgcn_mfma_f32_16x16x32_bf16(af[k], b3[8 * hf + k], dacc, 0, 0, 0);
        d2 = __builtin_amdgcn_mfma_f32_16x16x32_bf16(af[k + 1], b3[8 * hf + k + 1], d2, 0, 0, 0);
      }
      __builtin_amdgcn_sched_barrier(0);
    }
    dacc += d2;
    if (lane < 32) {
      const int col = 16 * wave + m16;
      u64* og = p.ws + OG_OFF + ((((size_t)cg * 8 + (col >> 3)) * 8 + rg) * 8) * 8 + (col & 7);
#pragma unroll
      for (int i = 0; i < 4; ++i) put_gran(og + (4 * g4 + i) * 8, __float_as_uint(dacc[i]));
    }
  }
  if (wave == 0) STAMP(14);
  if (wave == 0) {
    const u64* base = p.ws + OG_OFF + (((size_t)cg * 8 + rg) * 8) * 64 + lane;
    u64 a[8];
    for (u32 it = 0; it < SPIN_CAP; ++it) {
      bool ok = true;
#pragma unroll
      for (int w = 0; w < 8; ++w) a[w] = get_gran(base + w * 64);
#pragma unroll
      for (int w = 0; w < 8; ++w) ok = ok && tag_ok(a[w]);
      if (__all(ok)) break;
      __builtin_amdgcn_s_sleep(1);
    }
    STAMP(15);
    float r = bf2f(xr0) + bf2f(xr1);
#pragma unroll
    for (int w = 0; w < 8; ++w) r += __uint_as_float((u32)a[w]);
    p.out[(lane >> 3) * DM + ocol + (lane & 7)] = (u16)f2bf(r);
    if (j == 0 && lane == 0) __hip_atomic_store((GAS u32*)(p.ws + EP_OFF), TAG, RLX_AGENT);
    STAMP(16);
  }
}

}

extern "C" void kernel_launch(void* const* d_in, const int* in_sizes, int n_in,
                              void* d_out, int out_size, void* d_ws, size_t ws_size,
                              hipStream_t stream) {
  Params p;
  p.x = (const u16*)d_in[0];
  p.kc = (const u16*)d_in[1];
  p.vc = (const u16*)d_in[2];
  p.wq = (const u16*)d_in[3];
  p.wo = (const u16*)d_in[4];
  p.w1 = (const u16*)d_in[6];
  p.b1 = (const u16*)d_in[7];
  p.w2 = (const u16*)d_in[8];
  p.b2 = (const u16*)d_in[9];
  p.out = (u16*)d_out;
  p.ws = (u64*)d_ws;
  hipLaunchKernelGGL(fused_decode, dim3(NWG), dim3(NTHR), 0, stream, p);
}
```

```cpp
#include <hip/hip_runtime.h>
#include <hip/hip_bf16.h>
#include <stdint.h>

typedef unsigned int u32;
typedef unsigned long long u64;
typedef unsigned short u16;
typedef u32 u32x4 __attribute__((ext_vector_type(4)));
typedef float f32x4 __attribute__((ext_vector_type(4)));
typedef short bf16x8 __attribute__((ext_vector_type(8)));
typedef short s16x4 __attribute__((ext_vector_type(4)));

#define GAS __attribute__((address_space(1)))
#define LAS __attribute__((address_space(3)))
#define RLX_AGENT __ATOMIC_RELAXED, __HIP_MEMORY_SCOPE_AGENT

namespace {

constexpr int NB = 8, DM = 512, HQ = 32, HKV = 8, SK = 4096, DH = 128, WINDOW = 1024, FF = 1792;
constexpr int KOFF = SK - WINDOW;
constexpr int NWG = 256, NTHR = 1024;
constexpr u32 SPIN_CAP = 1u << 22;
constexpr float LOG2E = 1.4426950408889634f;

constexpr int NSP = 3;
constexpr int PREC = 66;
constexpr size_t QG_OFF = 0, QG_N = (size_t)NB * 2048;
constexpr size_t HG_OFF = QG_OFF + QG_N, HG_N = (size_t)NB * 2 * FF;
constexpr size_t PG_OFF = HG_OFF + HG_N, PG_N = (size_t)HQ * NB * NSP * PREC;
constexpr size_t OG_OFF = PG_OFF + PG_N, OG_N = (size_t)8 * 8 * 8 * 8 * 8;
constexpr size_t EP_OFF = OG_OFF + OG_N;
constexpr size_t WS_GRAN = EP_OFF + 16;
constexpr size_t WS_BYTES = WS_GRAN * 8;
static_assert(WS_BYTES % 16 == 0, "memset block must be a multiple of 16 bytes");

constexpr int L_CTRL = 0;
constexpr int L_Q = 64;
constexpr int L_ML = L_Q + 1024;
constexpr int L_SC = L_ML + 512;
constexpr int L_PB = L_SC + 2048;
constexpr int L_V = L_PB + 11 * 1024;
constexpr int LA_END = L_V + 352 * 256;
constexpr int XN_STRIDE = 520;
constexpr int K3 = 736, K3_STRIDE = 744;
constexpr int L_XN = 64;
constexpr int L_A3 = L_XN + NB * XN_STRIDE * 2;
constexpr int L_RED = L_A3 + NB * K3_STRIDE * 2;
constexpr int L_WQ = L_RED + 2048;
constexpr int L_W1 = L_WQ + 65536;
constexpr int L_W3 = L_WQ;
constexpr int L_PD = L_W1 + 32768;
constexpr int LB_END = L_PD + 16384;
static_assert(L_W3 + K3 * 128 <= LB_END, "W3 overlay");
constexpr int LDS_BYTES = LA_END > LB_END ? LA_END : LB_END;
static_assert(LDS_BYTES > 80 * 1024 && LDS_BYTES <= 160 * 1024, "one workgroup per CU");
static_assert(L_WQ % 16 == 0 && L_A3 % 16 == 0 && L_V % 16 == 0 && L_PB % 16 == 0 && L_RED % 16 == 0, "align");

struct Params {
  const u16* x; const u16* kc; const u16* vc; const u16* wq; const u16* wo;
  const u16* w1; const u16* b1; const u16* w2; const u16* b2;
  u16* out; u64* ws;
};

__device__ __forceinline__ float bf2f(u32 h) { return __uint_as_float(h << 16); }
__device__ __forceinline__ u32 f2bf(float f) {
  __bf16 h = (__bf16)f;
  return (u32)__builtin_bit_cast(u16, h);
}
__device__ __forceinline__ float bfr(float f) { return bf2f(f2bf(f)); }

template <int CTRL> __device__ __forceinline__ float dpp(float x) {
  return __builtin_bit_cast(float, __builtin_amdgcn_mov_dpp(__builtin_bit_cast(int, x), CTRL, 0xf, 0xf, true));
}
template <int CTRL> __device__ __forceinline__ u32 dppu(u32 x) {
  return (u32)__builtin_amdgcn_mov_dpp((int)x, CTRL, 0xf, 0xf, true);
}
__device__ __forceinline__ float x16_max(float x) {
  auto s = __builtin_amdgcn_permlane16_swap(__float_as_uint(x), __float_as_uint(x), false, false);
  return fmaxf(__uint_as_float(s[0]), __uint_as_float(s[1]));
}
__device__ __forceinline__ float x32_max(float x) {
  auto t = __builtin_amdgcn_permlane32_swap(__float_as_uint(x), __float_as_uint(x), false, false);
  return fmaxf(__uint_as_float(t[0]), __uint_as_float(t[1]));
}
__device__ __forceinline__ float x16_sum(float x) {
  auto s = __builtin_amdgcn_permlane16_swap(__float_as_uint(x), __float_as_uint(x), false, false);
  return __uint_as_float(s[0]) + __uint_as_float(s[1]);
}
__device__ __forceinline__ float x32_sum(float x) {
  auto t = __builtin_amdgcn_permlane32_swap(__float_as_uint(x), __float_as_uint(x), false, false);
  return __uint_as_float(t[0]) + __uint_as_float(t[1]);
}

__device__ __forceinline__ void put_gran_t(u64* g, u32 v, u32 tag) {
  __hip_atomic_store((GAS u64*)g, ((u64)tag << 32) | (u64)v, RLX_AGENT);
}
__device__ __forceinline__ u64 get_gran(const u64* g) {
  return __hip_atomic_load((GAS u64*)g, RLX_AGENT);
}
#define tag_ok(v) ((u32)((v) >> 32) == TAG)
#define put_gran(g, v) put_gran_t((g), (v), TAG)
__device__ __forceinline__ u32x4 get_gran2(__amdgpu_buffer_rsrc_t rs, size_t gidx) {
  return __builtin_amdgcn_raw_buffer_load_b128(rs, (int)(gidx * 8), 0, 16);
}

__device__ __forceinline__ void lds_wait() { asm volatile("s_waitcnt lgkmcnt(0)" ::: "memory"); }
__device__ __forceinline__ void vm_wait0() { asm volatile("s_waitcnt vmcnt(0)" ::: "memory"); }

typedef volatile LAS u32 lctr;
__device__ __forceinline__ void lds_add1(lctr* c) {
  __hip_atomic_fetch_add((LAS u32*)c, 1u, __ATOMIC_RELAXED, __HIP_MEMORY_SCOPE_WORKGROUP);
}
__device__ __forceinline__ void lds_arrive(lctr* c, int lane) {
  lds_wait();
  if (lane == 0) lds_add1(c);
  asm volatile("" ::: "memory");
}
__device__ __forceinline__ void lds_spin(lctr* c, u32 target) {
  for (u32 it = 0; it < SPIN_CAP; ++it) {
    if (*c >= target) break;
    __builtin_amdgcn_s_sleep(1);
  }
  asm volatile("" ::: "memory");
}

__device__ __forceinline__ float gelu_tanh(float v) {
  const float u = 0.7978845608028654f * (v + 0.044715f * v * v * v);
  return 0.5f * v * (1.f + tanhf(u));
}

__device__ __forceinline__ void dma16(const void* gsrc, unsigned char* lds_base) {
  __builtin_amdgcn_global_load_lds(gsrc, (LAS void*)lds_base, 16, 0, 2);
}
__device__ __forceinline__ int wswz(int row) { return (((row >> 1) & 1) | (((row >> 3) & 1) << 1)) << 1; }
__device__ __forceinline__ bf16x8 bfrag_tr(const unsigned char* tile, int ks, int nt, int lane) {
  const int g = lane >> 4, q = (lane & 15) >> 2, pp = lane & 3;
  const int row = 8 * g + q;
  const unsigned char* a = tile + (32 * ks + row) * 128 + (((2 * nt + (pp >> 1)) ^ wswz(row)) * 16) + 8 * (pp & 1);
  const s16x4 lo = __builtin_amdgcn_ds_read_tr16_b64_v4i16((LAS s16x4*)a);
  const s16x4 hi = __builtin_amdgcn_ds_read_tr16_b64_v4i16((LAS s16x4*)(a + 4 * 128));
  return bf16x8{lo[0], lo[1], lo[2], lo[3], hi[0], hi[1], hi[2], hi[3]};
}
__device__ __forceinline__ int vswz(int row) { return ((row & 3) << 2) | ((row >> 2) & 3); }

#define KV_WIN 5
#define Q_SLEEP 8
#define PG_SLEEP 4
#define STAMP(k) do { } while (0)

__global__ void __launch_bounds__(NTHR, 4) fused_decode(Params p) {
  __shared__ __attribute__((aligned(16))) unsigned char smem[LDS_BYTES];
  lctr* ctrl = (lctr*)(smem + L_CTRL);

  const int tid = threadIdx.x, lane = tid & 63;
  const int wave = __builtin_amdgcn_readfirstlane(tid >> 6);
  const int bid = blockIdx.x, xg = bid & 7, y = bid >> 3;
  const int g4 = lane >> 4, m16 = lane & 15;

  if (y < 24) {
    const int hkv = xg, bb = y / 3, sp = y - 3 * bb;
    const int nkw = sp == 2 ? 10 : 11;
    if (wave > 11 || (wave == 10 && nkw == 10)) return;
    if (wave == 11) {
      const u32 TAG = __hip_atomic_load((GAS u32*)(p.ws + EP_OFF), RLX_AGENT) + 1u;
      if (lane < 16) ctrl[lane] = 0u;
      lds_wait();
      __builtin_amdgcn_s_barrier();
      STAMP(6);
      const u64* g = p.ws + QG_OFF + (size_t)bb * 2048 + hkv * 256 + 4 * lane;
      u64 v[4];
      for (u32 it = 0; it < SPIN_CAP; ++it) {
#pragma unroll
        for (int k = 0; k < 4; ++k) v[k] = get_gran(g + k);
        if (__all(tag_ok(v[0]) && tag_ok(v[1]) && tag_ok(v[2]) && tag_ok(v[3]))) break;
        __builtin_amdgcn_s_sleep(Q_SLEEP);
      }
      *(u32x4*)(smem + L_Q + lane * 16) = u32x4{(u32)v[0], (u32)v[1], (u32)v[2], (u32)v[3]};
      if (lane == 0) ctrl[8] = TAG;
      lds_arrive(&ctrl[0], lane);
      STAMP(7);
      return;
    }
    if (wave == 0) STAMP(0);
    const size_t kvbase = ((size_t)(bb * HKV + hkv) * SK + KOFF + sp * 352 + wave * 32) * DH;
    const u32x4* kp = (const u32x4*)(p.kc + kvbase + (size_t)m16 * DH + g4 * 8);
    u32x4 kf[8];
#define KV_ISSUE(n) do { \
      if ((n) < 8) { asm volatile("global_load_dwordx4 %0, %1, off nt" : "=v"(kf[(n) & 7]) : "v"(kp + ((n) >> 2) * 16 * (DH / 8) + ((n) & 3) * 4) : "memory"); } \
      else { const int row_ = 32 * wave + 4 * ((n) - 8) + g4; \
             dma16(p.vc + kvbase + (size_t)(4 * ((n) - 8) + g4) * DH + ((m16 ^ vswz(row_)) * 8), smem + L_V + (32 * wave + 4 * ((n) - 8)) * 256); } \
      asm volatile("" ::: "memory"); } while (0)
#pragma unroll
    for (int n = 0; n < KV_WIN; ++n) KV_ISSUE(n);
    __builtin_amdgcn_s_barrier();
#pragma unroll
    for (int n = KV_WIN; n < 16; ++n) {
      asm volatile("s_waitcnt vmcnt(%0)" :: "i"(KV_WIN - 1) : "memory");
      KV_ISSUE(n);
    }
#undef KV_ISSUE
    asm volatile("s_waitcnt vmcnt(8)" : "+v"(kf[0]), "+v"(kf[1]), "+v"(kf[2]), "+v"(kf[3]), "+v"(kf[4]), "+v"(kf[5]), "+v"(kf[6]), "+v"(kf[7]) :: "memory");
    if (wave == 0) STAMP(1);
    lds_spin(&ctrl[0], 1);
    const u32 TAG = ctrl[8];
    if (wave == 0) STAMP(2);
    bf16x8 qf[4];
#pragma unroll
    for (int t = 0; t < 4; ++t) {
      bf16x8 z = {0, 0, 0, 0, 0, 0, 0, 0};
      if (m16 < 4) z = *(const bf16x8*)(smem + L_Q + (m16 * 128 + t * 32 + g4 * 8) * 2);
      qf[t] = z;
    }
    constexpr float SCALE = 0.08838834764831845f;
    float a[8];
#pragma unroll
    for (int T = 0; T < 2; ++T) {
      f32x4 s = {0.f, 0.f, 0.f, 0.f};
#pragma unroll
      for (int t = 0; t < 4; ++t) s = __builtin_amdgcn_mfma_f32_16x16x32_bf16(__builtin_bit_cast(bf16x8, kf[4 * T + t]), qf[t], s, 0, 0, 0);
#pragma unroll
      for (int i = 0; i < 4; ++i) a[4 * T + i] = bfr(bfr(s[i]) * SCALE);
    }
    float m = a[0];
#pragma unroll
    for (int i = 1; i < 8; ++i) m = fmaxf(m, a[i]);
    m = x32_max(x16_max(m));
    if (lane < 4) ((float*)(smem + L_ML))[wave * 4 + lane] = m;
    lds_arrive(&ctrl[2], lane);
    lds_spin(&ctrl[2], (u32)nkw);
    float M = m;
#pragma unroll
    for (int w = 0; w < 11; ++w)
      if (w < 10 || nkw == 11) M = fmaxf(M, ((const float*)(smem + L_ML))[w * 4 + (m16 & 3)]);
    const float Ms = M * LOG2E;
    float l = 0.f;
    u32x4 pf;
#pragma unroll
    for (int i = 0; i < 4; ++i) {
      const u32 lo = f2bf(__builtin_amdgcn_exp2f(fmaf(a[2 * i], LOG2E, -Ms))), hi = f2bf(__builtin_amdgcn_exp2f(fmaf(a[2 * i + 1], LOG2E, -Ms)));
      pf[i] = lo | (hi << 16);
      l += bf2f(lo) + bf2f(hi);
    }
    l = x32_sum(x16_sum(l));
    *(u32x4*)(smem + L_PB + (wave * 64 + lane) * 16) = pf;
    if (lane < 4) ((float*)(smem + L_SC))[wave * 4 + lane] = l;
    if (wave == 0) STAMP(3);
    vm_wait0();
    lds_arrive(&ctrl[1], lane);
    if (wave >= 8) return;
    lds_spin(&ctrl[1], (u32)nkw);
    if (wave == 0) STAMP(4);
    f32x4 o = {0.f, 0.f, 0.f, 0.f};
    {
      const int q = m16 >> 2, pp = lane & 3;
      const int ch = 2 * wave + (pp >> 1);
      bf16x8 pa[11], vb[11];
#pragma unroll
      for (int w = 0; w < 11; ++w) {
        if (w < 10 || nkw == 11) {
          pa[w] = *(const bf16x8*)(smem + L_PB + (w * 64 + lane) * 16);
          const int r0 = 32 * w + 4 * g4 + q, r1 = r0 + 16;
          const s16x4 lo = __builtin_amdgcn_ds_read_tr16_b64_v4i16((LAS s16x4*)(smem + L_V + r0 * 256 + ((ch ^ vswz(r0)) * 16) + 8 * (pp & 1)));
          const s16x4 hi = __builtin_amdgcn_ds_read_tr16_b64_v4i16((LAS s16x4*)(smem + L_V + r1 * 256 + ((ch ^ vswz(r1)) * 16) + 8 * (pp & 1)));
          vb[w] = bf16x8{lo[0], lo[1], lo[2], lo[3], hi[0], hi[1], hi[2], hi[3]};
        }
      }
      f32x4 o2 = {0.f, 0.f, 0.f, 0.f};
#pragma unroll
      for (int w = 0; w < 10; w += 2) {
        o = __builtin_amdgcn_mfma_f32_16x16x32_bf16(pa[w], vb[w], o, 0, 0, 0);
        o2 = __builtin_amdgcn_mfma_f32_16x16x32_bf16(pa[w + 1], vb[w + 1], o2, 0, 0, 0);
      }
      if (nkw == 11) o = __builtin_amdgcn_mfma_f32_16x16x32_bf16(pa[10], vb[10], o, 0, 0, 0);
      o += o2;
    }
    {
      u64* rec = p.ws + PG_OFF + (((size_t)(hkv * 4) * NB + bb) * NSP + sp) * PREC;
#pragma unroll
      for (int h = 0; h < 4; ++h) {
        const u32 mine = f2bf(o[h]);
        const u32 other = dppu<0xB1>(mine);
        if (lane < 16 && (lane & 1) == 0) put_gran(rec + (size_t)h * NB * NSP * PREC + 8 * wave + (lane >> 1), mine | (other << 16));
      }
      if (wave == 0 && lane < 4) {
        float L = 0.f;
#pragma unroll
        for (int w = 0; w < 11; ++w)
          if (w < 10 || nkw == 11) L += ((const float*)(smem + L_SC))[w * 4 + lane];
        put_gran(rec + (size_t)lane * NB * NSP * PREC + 64, __float_as_uint(M));
        put_gran(rec + (size_t)lane * NB * NSP * PREC + 65, __float_as_uint(L));
      }
    }
    if (wave == 0) STAMP(5);
    return;
  }

  const int j = xg + 8 * (y - 24);
  const u32 TAG = __hip_atomic_load((GAS u32*)(p.ws + EP_OFF), RLX_AGENT) + 1u;
  if (wave == 0) STAMP(8);
  if (tid < 16) ctrl[tid] = 0u;
  const bool has_ffn = j < 56;
  const int fcb = j % 28, fks = j / 28;
  const int rg = j >> 3, cg = j & 7;

  const int ocol = 64 * cg + 8 * rg;
  u32 bias = 0;
  if (tid < 896) bias = *(const u32*)(p.b1 + 224 * rg + 2 * (tid % 112));
  u32x4 xa = {0, 0, 0, 0};
  if (wave < 8) xa = *(const u32x4*)(p.x + wave * DM + lane * 8);
#pragma unroll
  for (int i = 0; i < 4; ++i) {
    const int r0 = 128 * i + 8 * wave;
    dma16(p.wq + (size_t)(r0 + (lane >> 3)) * 4096 + 64 * j + 8 * ((lane & 7) ^ wswz(r0 + (lane >> 3))), smem + L_WQ + r0 * 128);
  }
  if (has_ffn) {
#pragma unroll
    for (int i = 0; i < 2; ++i) {
      const int r0 = 128 * i + 8 * wave;
      dma16(p.w1 + (size_t)(256 * fks + r0 + (lane >> 3)) * FF + 64 * fcb + 8 * ((lane & 7) ^ wswz(r0 + (lane >> 3))), smem + L_W1 + r0 * 128);
    }
  }
  if (wave < 8) {
    float xv[8];
#pragma unroll
    for (int k = 0; k < 4; ++k) { xv[2 * k] = __uint_as_float(xa[k] << 16); xv[2 * k + 1] = __uint_as_float(xa[k] & 0xffff0000u); }
    float s = 0.f;
#pragma unroll
    for (int k = 0; k < 8; ++k) s += xv[k];
    s += dpp<0x121>(s); s += dpp<0x122>(s); s += dpp<0x124>(s); s += dpp<0x128>(s); s = x32_sum(x16_sum(s));
    const float mean = s * (1.f / 512.f);
    float v = 0.f;
#pragma unroll
    for (int k = 0; k < 8; ++k) { const float d = xv[k] - mean; v = fmaf(d, d, v); }
    v += dpp<0x121>(v); v += dpp<0x122>(v); v += dpp<0x124>(v); v += dpp<0x128>(v); v = x32_sum(x16_sum(v));
    const float mb = bfr(mean), vb = bfr(v * (1.f / 512.f));
    const float den = bfr(sqrtf(bfr(vb + 1e-5f)));
    u32x4 o;
#pragma unroll
    for (int k = 0; k < 4; ++k) {
      const float a = bfr(xv[2 * k] - mb) / den, b = bfr(xv[2 * k + 1] - mb) / den;
      o[k] = f2bf(a) | (f2bf(b) << 16);
    }
    *(u32x4*)(smem + L_XN + (wave * XN_STRIDE + lane * 8) * 2) = o;
  }
  vm_wait0();
  lds_wait();
  __builtin_amdgcn_s_barrier();
  if (wave == 0) STAMP(9);
  {
    const int nt = wave & 3, kq = wave >> 2;
    bf16x8 af[4], bq[4], ag[2], bg[2];
#pragma unroll
    for (int k = 0; k < 4; ++k) {
      bf16x8 a = {0, 0, 0, 0, 0, 0, 0, 0};
      if (m16 < 8) a = *(const bf16x8*)(smem + L_XN + (m16 * XN_STRIDE + (4 * kq + k) * 32 + g4 * 8) * 2);
      af[k] = a;
      bq[k] = bfrag_tr(smem + L_WQ, 4 * kq + k, nt, lane);
    }
    if (has_ffn) {
#pragma unroll
      for (int k = 0; k < 2; ++k) {
        bf16x8 a = {0, 0, 0, 0, 0, 0, 0, 0};
        if (m16 < 8) a = *(const bf16x8*)(smem + L_XN + (m16 * XN_STRIDE + 256 * fks + (2 * kq + k) * 32 + g4 * 8) * 2);
        ag[k] = a;
        bg[k] = bfrag_tr(smem + L_W1, 2 * kq + k, nt, lane);
      }
    }
    f32x4 d = {0.f, 0.f, 0.f, 0.f};
#pragma unroll
    for (int k = 0; k < 4; ++k) d = __builtin_amdgcn_mfma_f32_16x16x32_bf16(af[k], bq[k], d, 0, 0, 0);
    if (lane < 32) *(f32x4*)(smem + L_PD + ((kq * 4 + nt) * 32 + lane) * 16) = d;
    if (has_ffn) {
      f32x4 e = {0.f, 0.f, 0.f, 0.f};
#pragma unroll
      for (int k = 0; k < 2; ++k) e = __builtin_amdgcn_mfma_f32_16x16x32_bf16(ag[k], bg[k], e, 0, 0, 0);
      if (lane < 32) *(f32x4*)(smem + L_PD + 8192 + ((kq * 4 + nt) * 32 + lane) * 16) = e;
    }
  }
  if (wave == 0) STAMP(19);
  lds_wait();
  __builtin_amdgcn_s_barrier();
  if (wave == 0) STAMP(20);
  if (wave < 4) {
    f32x4 d = {0.f, 0.f, 0.f, 0.f};
    if (lane < 32) {
#pragma unroll
      for (int kq = 0; kq < 4; ++kq) d += *(const f32x4*)(smem + L_PD + ((kq * 4 + wave) * 32 + lane) * 16);
    }
#pragma unroll
    for (int i = 0; i < 4; ++i) {
      const u32 mine = f2bf(d[i]);
      const u32 other = dppu<0xB1>(mine);
      if (lane < 32 && (lane & 1) == 0)
        put_gran(p.ws + QG_OFF + (size_t)(4 * g4 + i) * 2048 + 32 * j + 8 * wave + (m16 >> 1), mine | (other << 16));
    }
  } else if (wave < 8 && has_ffn) {
    if (lane < 32) {
      f32x4 e = {0.f, 0.f, 0.f, 0.f};
#pragma unroll
      for (int kq = 0; kq < 4; ++kq) e += *(const f32x4*)(smem + L_PD + 8192 + ((kq * 4 + wave - 4) * 32 + lane) * 16);
#pragma unroll
      for (int i = 0; i < 4; ++i)
        put_gran(p.ws + HG_OFF + ((size_t)(4 * g4 + i) * 2 + fks) * FF + 64 * fcb + 16 * (wave - 4) + m16, __float_as_uint(e[i]));
    }
  }
  if (wave < 8) lds_arrive(&ctrl[7], lane);
  u32 xr0 = 0, xr1 = 0;
  if (tid < 64) { xr0 = p.x[(tid >> 3) * DM + ocol + (tid & 7)]; xr1 = p.b2[ocol + (tid & 7)]; }
  if (wave == 0) STAMP(10);
  if (wave >= 14) {
    lds_spin(&ctrl[7], 8);
#pragma unroll
    for (int i = 0; i < 46; ++i) {
      const int r0 = 16 * i + 8 * (wave - 14);
      const int r = r0 + (lane >> 3);
      const u16* src = r0 < 512 ? (p.wo + (size_t)(512 * rg + r) * DM) : (p.w2 + (size_t)(224 * rg + r - 512) * DM);
      dma16(src + 64 * cg + 8 * ((lane & 7) ^ wswz(r)), smem + L_W3 + r0 * 128);
    }
    vm_wait0();
    lds_arrive(&ctrl[5], lane);
  }
  if (tid < 896) {
    const int b = tid / 112, jj = tid - 112 * b;
    const u64* g = p.ws + HG_OFF + (size_t)b * 2 * FF + 224 * rg + 2 * jj;
    u64 v[4];
    for (u32 it = 0; it < SPIN_CAP; ++it) {
      v[0] = get_gran(g); v[1] = get_gran(g + 1); v[2] = get_gran(g + FF); v[3] = get_gran(g + FF + 1);
      if (tag_ok(v[0]) && tag_ok(v[1]) && tag_ok(v[2]) && tag_ok(v[3])) break;
      __builtin_amdgcn_s_sleep(4);
    }
    float h0 = __uint_as_float((u32)v[0]) + __uint_as_float((u32)v[2]);
    float h1 = __uint_as_float((u32)v[1]) + __uint_as_float((u32)v[3]);
    h0 = gelu_tanh(bfr(bfr(h0) + bf2f(bias & 0xffffu)));
    h1 = gelu_tanh(bfr(bfr(h1) + bf2f(bias >> 16)));
    ((u32*)(smem + L_A3))[b * (K3_STRIDE / 2) + 256 + jj] = f2bf(h0) | (f2bf(h1) << 16);
  }
  lds_arrive(&ctrl[6], lane);
  lds_spin(&ctrl[6], 16);
  lds_spin(&ctrl[5], 2);
  f32x4 dacc = {0.f, 0.f, 0.f, 0.f};
  bf16x8 b3[16];
  if (wave < 4) {
#pragma unroll
    for (int ks = 16; ks < 23; ++ks) {
      bf16x8 a = {0, 0, 0, 0, 0, 0, 0, 0};
      if (m16 < 8) a = *(const bf16x8*)(smem + L_A3 + (m16 * K3_STRIDE + ks * 32 + g4 * 8) * 2);
      dacc = __builtin_amdgcn_mfma_f32_16x16x32_bf16(a, bfrag_tr(smem + L_W3, ks, wave, lane), dacc, 0, 0, 0);
    }
#pragma unroll
    for (int k = 0; k < 16; ++k) b3[k] = bfrag_tr(smem + L_W3, k, wave, lane);
  }
  if (wave == 0) STAMP(11);
  {
    const int hh = tid >> 8, b = (tid >> 5) & 7, jj = tid & 31;
    const size_t base = PG_OFF + ((size_t)(4 * rg + hh) * NB + b) * NSP * PREC;
    const __amdgpu_buffer_rsrc_t rs = __builtin_amdgcn_make_buffer_rsrc((void*)p.ws, 0, (int)WS_BYTES, 0x00020000);
    u32x4 ov[NSP], ml[NSP];
    for (u32 it = 0; it < SPIN_CAP; ++it) {
      bool ok = true;
#pragma unroll
      for (int s = 0; s < NSP; ++s) {
        ov[s] = get_gran2(rs, base + s * PREC + 2 * jj);
        ml[s] = get_gran2(rs, base + s * PREC + 64);
      }
#pragma unroll
      for (int s = 0; s < NSP; ++s) ok = ok && ov[s][1] == TAG && ov[s][3] == TAG && ml[s][1] == TAG && ml[s][3] == TAG;
      if (__all(ok)) break;
      __builtin_amdgcn_s_sleep(PG_SLEEP);
    }
    float M = -INFINITY;
#pragma unroll
    for (int s = 0; s < NSP; ++s) M = fmaxf(M, __uint_as_float(ml[s][0]));
    float L = 0.f, o[4] = {0.f, 0.f, 0.f, 0.f};
#pragma unroll
    for (int s = 0; s < NSP; ++s) {
      const float wt = __builtin_amdgcn_exp2f((__uint_as_float(ml[s][0]) - M) * LOG2E);
      L = fmaf(wt, __uint_as_float(ml[s][2]), L);
      o[0] = fmaf(wt, bf2f(ov[s][0] & 0xffffu), o[0]);
      o[1] = fmaf(wt, bf2f(ov[s][0] >> 16), o[1]);
      o[2] = fmaf(wt, bf2f(ov[s][2] & 0xffffu), o[2]);
      o[3] = fmaf(wt, bf2f(ov[s][2] >> 16), o[3]);
    }
    const float inv = 1.f / L;
    u32* a3 = (u32*)(smem + L_A3) + b * (K3_STRIDE / 2) + 64 * hh + 2 * jj;
    a3[0] = f2bf(o[0] * inv) | (f2bf(o[1] * inv) << 16);
    a3[1] = f2bf(o[2] * inv) | (f2bf(o[3] * inv) << 16);
  }
  if (wave == 0) STAMP(12);
  lds_wait();
  __builtin_amdgcn_s_barrier();
  if (wave == 0) STAMP(13);
  if (wave < 4) {
    f32x4 d2 = {0.f, 0.f, 0.f, 0.f};
#pragma unroll
    for (int hf = 0; hf < 2; ++hf) {
      bf16x8 af[8];
#pragma unroll
      for (int k = 0; k < 8; ++k) {
        bf16x8 a = {0, 0, 0, 0, 0, 0, 0, 0};
        if (m16 < 8) a = *(const bf16x8*)(smem + L_A3 + (m16 * K3_STRIDE + (8 * hf + k) * 32 + g4 * 8) * 2);
        af[k] = a;
      }
#pragma unroll
      for (int k = 0; k < 8; k += 2) {
        dacc = __builtin_amdgcn_mfma_f32_16x16x32_bf16(af[k], b3[8 * hf + k], dacc, 0, 0, 0);
        d2 = __builtin_amdgcn_mfma_f32_16x16x32_bf16(af[k + 1], b3[8 * hf + k + 1], d2, 0, 0, 0);
      }
      __builtin_amdgcn_sched_barrier(0);
    }
    dacc += d2;
    if (lane < 32) {
      const int col = 16 * wave + m16;
      u64* og = p.ws + OG_OFF + ((((size_t)cg * 8 + (col >> 3)) * 8 + rg) * 8) * 8 + (col & 7);
#pragma unroll
      for (int i = 0; i < 4; ++i) put_gran(og + (4 * g4 + i) * 8, __float_as_uint(dacc[i]));
    }
  }
  if (wave == 0) STAMP(14);
  if (wave == 0) {
    const u64* base = p.ws + OG_OFF + (((size_t)cg * 8 + rg) * 8) * 64 + lane;
    u64 a[8];
    for (u32 it = 0; it < SPIN_CAP; ++it) {
      bool ok = true;
#pragma unroll
      for (int w = 0; w < 8; ++w) a[w] = get_gran(base + w * 64);
#pragma unroll
      for (int w = 0; w < 8; ++w) ok = ok && tag_ok(a[w]);
      if (__all(ok)) break;
      __builtin_amdgcn_s_sleep(1);
    }
    STAMP(15);
    float r = bf2f(xr0) + bf2f(xr1);
#pragma unroll
    for (int w = 0; w < 8; ++w) r += __uint_as_float((u32)a[w]);
    p.out[(lane >> 3) * DM + ocol + (lane & 7)] = (u16)f2bf(r);
    if (j == 0 && lane == 0) __hip_atomic_store((GAS u32*)(p.ws + EP_OFF), TAG, RLX_AGENT);
    STAMP(16);
  }
}

}

extern "C" void kernel_launch(void* const* d_in, const int* in_sizes, int n_in,
                              void* d_out, int out_size, void* d_ws, size_t ws_size,
                              hipStream_t stream) {
  Params p;
  p.x = (const u16*)d_in[0];
  p.kc = (const u16*)d_in[1];
  p.vc = (const u16*)d_in[2];
  p.wq = (const u16*)d_in[3];
  p.wo = (const u16*)d_in[4];
  p.w1 = (const u16*)d_in[6];
  p.b1 = (const u16*)d_in[7];
  p.w2 = (const u16*)d_in[8];
  p.b2 = (const u16*)d_in[9];
  p.out = (u16*)d_out;
  p.ws = (u64*)d_ws;
  hipLaunchKernelGGL(fused_decode, dim3(NWG), dim3(NTHR), 0, stream, p);
}
```

```cpp
#include <hip/hip_runtime.h>
#include <hip/hip_bf16.h>
#include <stdint.h>

typedef unsigned int u32;
typedef unsigned long long u64;
typedef unsigned short u16;
typedef u32 u32x4 __attribute__((ext_vector_type(4)));
typedef float f32x4 __attribute__((ext_vector_type(4)));
typedef short bf16x8 __attribute__((ext_vector_type(8)));
typedef short s16x4 __attribute__((ext_vector_type(4)));

#define GAS __attribute__((address_space(1)))
#define LAS __attribute__((address_space(3)))
#define RLX_AGENT __ATOMIC_RELAXED, __HIP_MEMORY_SCOPE_AGENT

namespace {

constexpr int NB = 8, DM = 512, HQ = 32, HKV = 8, SK = 4096, DH = 128, WINDOW = 1024, FF = 1792;
constexpr int KOFF = SK - WINDOW;
constexpr int NWG = 256, NTHR = 1024;
constexpr u32 SPIN_CAP = 1u << 22;
constexpr float LOG2E = 1.4426950408889634f;

constexpr int NSP = 3;
constexpr int PREC = 66;
constexpr size_t QG_OFF = 0, QG_N = (size_t)NB * 2048;
constexpr size_t HG_OFF = QG_OFF + QG_N, HG_N = (size_t)NB * 2 * FF;
constexpr size_t PG_OFF = HG_OFF + HG_N, PG_N = (size_t)HQ * NB * NSP * PREC;
constexpr size_t OG_OFF = PG_OFF + PG_N, OG_N = (size_t)8 * 8 * 8 * 8 * 8;
constexpr size_t EP_OFF = OG_OFF + OG_N;
constexpr size_t WS_GRAN = EP_OFF + 16;
constexpr size_t WS_BYTES = WS_GRAN * 8;
static_assert(WS_BYTES % 16 == 0, "memset block must be a multiple of 16 bytes");

constexpr int L_CTRL = 0;
constexpr int L_Q = 64;
constexpr int L_ML = L_Q + 1024;
constexpr int L_SC = L_ML + 512;
constexpr int L_PB = L_SC + 2048;
constexpr int L_V = L_PB + 11 * 1024;
constexpr int LA_END = L_V + 352 * 256;
constexpr int XN_STRIDE = 520;
constexpr int K3 = 736, K3_STRIDE = 744;
constexpr int L_XN = 64;
constexpr int L_A3 = L_XN + NB * XN_STRIDE * 2;
constexpr int L_RED = L_A3 + NB * K3_STRIDE * 2;
constexpr int L_WQ = L_RED + 2048;
constexpr int L_W1 = L_WQ + 65536;
constexpr int L_W3 = L_WQ;
constexpr int L_PD = L_W1 + 32768;
constexpr int LB_END = L_PD + 16384;
static_assert(L_W3 + K3 * 128 <= LB_END, "W3 overlay");
constexpr int LDS_BYTES = LA_END > LB_END ? LA_END : LB_END;
static_assert(LDS_BYTES > 80 * 1024 && LDS_BYTES <= 160 * 1024, "one workgroup per CU");
static_assert(L_WQ % 16 == 0 && L_A3 % 16 == 0 && L_V % 16 == 0 && L_PB % 16 == 0 && L_RED % 16 == 0, "align");

struct Params {
  const u16* x; const u16* kc; const u16* vc; const u16* wq; const u16* wo;
  const u16* w1; const u16* b1; const u16* w2; const u16* b2;
  u16* out; u64* ws;
};

__device__ __forceinline__ float bf2f(u32 h) { return __uint_as_float(h << 16); }
__device__ __forceinline__ u32 f2bf(float f) {
  __bf16 h = (__bf16)f;
  return (u32)__builtin_bit_cast(u16, h);
}
__device__ __forceinline__ float bfr(float f) { return bf2f(f2bf(f)); }

template <int CTRL> __device__ __forceinline__ float dpp(float x) {
  return __builtin_bit_cast(float, __builtin_amdgcn_mov_dpp(__builtin_bit_cast(int, x), CTRL, 0xf, 0xf, true));
}
template <int CTRL> __device__ __forceinline__ u32 dppu(u32 x) {
  return (u32)__builtin_amdgcn_mov_dpp((int)x, CTRL, 0xf, 0xf, true);
}
__device__ __forceinline__ float x16_max(float x) {
  auto s = __builtin_amdgcn_permlane16_swap(__float_as_uint(x), __float_as_uint(x), false, false);
  return fmaxf(__uint_as_float(s[0]), __uint_as_float(s[1]));
}
__device__ __forceinline__ float x32_max(float x) {
  auto t = __builtin_amdgcn_permlane32_swap(__float_as_uint(x), __float_as_uint(x), false, false);
  return fmaxf(__uint_as_float(t[0]), __uint_as_float(t[1]));
}
__device__ __forceinline__ float x16_sum(float x) {
  auto s = __builtin_amdgcn_permlane16_swap(__float_as_uint(x), __float_as_uint(x), false, false);
  return __uint_as_float(s[0]) + __uint_as_float(s[1]);
}
__device__ __forceinline__ float x32_sum(float x) {
  auto t = __builtin_amdgcn_permlane32_swap(__float_as_uint(x), __float_as_uint(x), false, false);
  return __uint_as_float(t[0]) + __uint_as_float(t[1]);
}

__device__ __forceinline__ void put_gran_t(u64* g, u32 v, u32 tag) {
  __hip_atomic_store((GAS u64*)g, ((u64)tag << 32) | (u64)v, RLX_AGENT);
}
__device__ __forceinline__ u64 get_gran(const u64* g) {
  return __hip_atomic_load((GAS u64*)g, RLX_AGENT);
}
#define tag_ok(v) ((u32)((v) >> 32) == TAG)
#define put_gran(g, v) put_gran_t((g), (v), TAG)
__device__ __forceinline__ u32x4 get_gran2(__amdgpu_buffer_rsrc_t rs, size_t gidx) {
  return __builtin_amdgcn_raw_buffer_load_b128(rs, (int)(gidx * 8), 0, 16);
}

__device__ __forceinline__ void lds_wait() { asm volatile("s_waitcnt lgkmcnt(0)" ::: "memory"); }
__device__ __forceinline__ void vm_wait0() { asm volatile("s_waitcnt vmcnt(0)" ::: "memory"); }

typedef volatile LAS u32 lctr;
__device__ __forceinline__ void lds_add1(lctr* c) {
  __hip_atomic_fetch_add((LAS u32*)c, 1u, __ATOMIC_RELAXED, __HIP_MEMORY_SCOPE_WORKGROUP);
}
__device__ __forceinline__ void lds_arrive(lctr* c, int lane) {
  lds_wait();
  if (lane == 0) lds_add1(c);
  asm volatile("" ::: "memory");
}
__device__ __forceinline__ void lds_spin(lctr* c, u32 target) {
  for (u32 it = 0; it < SPIN_CAP; ++it) {
    if (*c >= target) break;
    __builtin_amdgcn_s_sleep(1);
  }
  asm volatile("" ::: "memory");
}

__device__ __forceinline__ float gelu_tanh(float v) {
  const float u = 0.7978845608028654f * (v + 0.044715f * v * v * v);
  return 0.5f * v * (1.f + tanhf(u));
}

__device__ __forceinline__ void dma16(const void* gsrc, unsigned char* lds_base) {
  __builtin_amdgcn_global_load_lds(gsrc, (LAS void*)lds_base, 16, 0, 2);
}
__device__ __forceinline__ int wswz(int row) { return (((row >> 1) & 1) | (((row >> 3) & 1) << 1)) << 1; }
__device__ __forceinline__ bf16x8 bfrag_tr(const unsigned char* tile, int ks, int nt, int lane) {
  const int g = lane >> 4, q = (lane & 15) >> 2, pp = lane & 3;
  const int row = 8 * g + q;
  const unsigned char* a = tile + (32 * ks + row) * 128 + (((2 * nt + (pp >> 1)) ^ wswz(row)) * 16) + 8 * (pp & 1);
  const s16x4 lo = __builtin_amdgcn_ds_read_tr16_b64_v4i16((LAS s16x4*)a);
  const s16x4 hi = __builtin_amdgcn_ds_read_tr16_b64_v4i16((LAS s16x4*)(a + 4 * 128));
  return bf16x8{lo[0], lo[1], lo[2], lo[3], hi[0], hi[1], hi[2], hi[3]};
}
__device__ __forceinline__ int vswz(int row) { return ((row & 3) << 2) | ((row >> 2) & 3); }

#define KV_WIN 5
#define Q_SLEEP 8
#define PG_SLEEP 4
#define STAMP(k) do { } while (0)

__global__ void __launch_bounds__(NTHR, 4) fused_decode(Params p) {
  __shared__ __attribute__((aligned(16))) unsigned char smem[LDS_BYTES];
  lctr* ctrl = (lctr*)(smem + L_CTRL);

  const int tid = threadIdx.x, lane = tid & 63;
  const int wave = __builtin_amdgcn_readfirstlane(tid >> 6);
  const int bid = blockIdx.x, xg = bid & 7, y = bid >> 3;
  const int g4 = lane >> 4, m16 = lane & 15;

  if (y < 24) {
    const int hkv = xg, bb = y / 3, sp = y - 3 * bb;
    const int nkw = sp == 2 ? 10 : 11;
    if (wave > 11 || (wave == 10 && nkw == 10)) return;
    if (wave == 11) {
      const u32 TAG = __hip_atomic_load((GAS u32*)(p.ws + EP_OFF), RLX_AGENT) + 1u;
      if (lane < 16) ctrl[lane] = 0u;
      lds_wait();
      __builtin_amdgcn_s_barrier();
      STAMP(6);
      const u64* g = p.ws + QG_OFF + (size_t)bb * 2048 + hkv * 256 + 4 * lane;
      u64 v[4];
      for (u32 it = 0; it < SPIN_CAP; ++it) {
#pragma unroll
        for (int k = 0; k < 4; ++k) v[k] = get_gran(g + k);
        if (__all(tag_ok(v[0]) && tag_ok(v[1]) && tag_ok(v[2]) && tag_ok(v[3]))) break;
        __builtin_amdgcn_s_sleep(Q_SLEEP);
      }
      *(u32x4*)(smem + L_Q + lane * 16) = u32x4{(u32)v[0], (u32)v[1], (u32)v[2], (u32)v[3]};
      if (lane == 0) ctrl[8] = TAG;
      lds_arrive(&ctrl[0], lane);
      STAMP(7);
      return;
    }
    if (wave == 0) STAMP(0);
    const size_t kvbase = ((size_t)(bb * HKV + hkv) * SK + KOFF + sp * 352 + wave * 32) * DH;
    const u32x4* kp = (const u32x4*)(p.kc + kvbase + (size_t)m16 * DH + g4 * 8);
    u32x4 kf[8];
#define KV_ISSUE(n) do { \
      if ((n) < 8) { asm volatile("global_load_dwordx4 %0, %1, off nt" : "=v"(kf[(n) & 7]) : "v"(kp + ((n) >> 2) * 16 * (DH / 8) + ((n) & 3) * 4) : "memory"); } \
      else { const int row_ = 32 * wave + 4 * ((n) - 8) + g4; \
             dma16(p.vc + kvbase + (size_t)(4 * ((n) - 8) + g4) * DH + ((m16 ^ vswz(row_)) * 8), smem + L_V + (32 * wave + 4 * ((n) - 8)) * 256); } \
      asm volatile("" ::: "memory"); } while (0)
#pragma unroll
    for (int n = 0; n < KV_WIN; ++n) KV_ISSUE(n);
    __builtin_amdgcn_s_barrier();
#pragma unroll
    for (int n = KV_WIN; n < 16; ++n) {
      asm volatile("s_waitcnt vmcnt(%0)" :: "i"(KV_WIN - 1) : "memory");
      KV_ISSUE(n);
    }
#undef KV_ISSUE
    asm volatile("s_waitcnt vmcnt(8)" : "+v"(kf[0]), "+v"(kf[1]), "+v"(kf[2]), "+v"(kf[3]), "+v"(kf[4]), "+v"(kf[5]), "+v"(kf[6]), "+v"(kf[7]) :: "memory");
    if (wave == 0) STAMP(1);
    lds_spin(&ctrl[0], 1);
    const u32 TAG = ctrl[8];
    if (wave == 0) STAMP(2);
    bf16x8 qf[4];
#pragma unroll
    for (int t = 0; t < 4; ++t) {
      bf16x8 z = {0, 0, 0, 0, 0, 0, 0, 0};
      if (m16 < 4) z = *(const bf16x8*)(smem + L_Q + (m16 * 128 + t * 32 + g4 * 8) * 2);
      qf[t] = z;
    }
    constexpr float SCALE = 0.08838834764831845f;
    float a[8];
#pragma unroll
    for (int T = 0; T < 2; ++T) {
      f32x4 s = {0.f, 0.f, 0.f, 0.f};
#pragma unroll
      for (int t = 0; t < 4; ++t) s = __builtin_amdgcn_mfma_f32_16x16x32_bf16(__builtin_bit_cast(bf16x8, kf[4 * T + t]), qf[t], s, 0, 0, 0);
#pragma unroll
      for (int i = 0; i < 4; ++i) a[4 * T + i] = bfr(bfr(s[i]) * SCALE);
    }
    float m = a[0];
#pragma unroll
    for (int i = 1; i < 8; ++i) m = fmaxf(m, a[i]);
    m = x32_max(x16_max(m));
    if (lane < 4) ((float*)(smem + L_ML))[wave * 4 + lane] = m;
    lds_arrive(&ctrl[2], lane);
    lds_spin(&ctrl[2], (u32)nkw);
    float M = m;
#pragma unroll
    for (int w = 0; w < 11; ++w)
      if (w < 10 || nkw == 11) M = fmaxf(M, ((const float*)(smem + L_ML))[w * 4 + (m16 & 3)]);
    const float Ms = M * LOG2E;
    float l = 0.f;
    u32x4 pf;
#pragma unroll
    for (int i = 0; i < 4; ++i) {
      const u32 lo = f2bf(__builtin_amdgcn_exp2f(fmaf(a[2 * i], LOG2E, -Ms))), hi = f2bf(__builtin_amdgcn_exp2f(fmaf(a[2 * i + 1], LOG2E, -Ms)));
      pf[i] = lo | (hi << 16);
      l += bf2f(lo) + bf2f(hi);
    }
    l = x32_sum(x16_sum(l));
    *(u32x4*)(smem + L_PB + (wave * 64 + lane) * 16) = pf;
    if (lane < 4) ((float*)(smem + L_SC))[wave * 4 + lane] = l;
    if (wave == 0) STAMP(3);
    vm_wait0();
    lds_arrive(&ctrl[1], lane);
    if (wave >= 8) return;
    lds_spin(&ctrl[1], (u32)nkw);
    if (wave == 0) STAMP(4);
    f32x4 o = {0.f, 0.f, 0.f, 0.f};
    {
      const int q = m16 >> 2, pp = lane & 3;
      const int ch = 2 * wave + (pp >> 1);
      bf16x8 pa[11], vb[11];
#pragma unroll
      for (int w = 0; w < 11; ++w) {
        if (w < 10 || nkw == 11) {
          pa[w] = *(const bf16x8*)(smem + L_PB + (w * 64 + lane) * 16);
          const int r0 = 32 * w + 4 * g4 + q, r1 = r0 + 16;
          const s16x4 lo = __builtin_amdgcn_ds_read_tr16_b64_v4i16((LAS s16x4*)(smem + L_V + r0 * 256 + ((ch ^ vswz(r0)) * 16) + 8 * (pp & 1)));
          const s16x4 hi = __builtin_amdgcn_ds_read_tr16_b64_v4i16((LAS s16x4*)(smem + L_V + r1 * 256 + ((ch ^ vswz(r1)) * 16) + 8 * (pp & 1)));
          vb[w] = bf16x8{lo[0], lo[1], lo[2], lo[3], hi[0], hi[1], hi[2], hi[3]};
        }
      }
      f32x4 o2 = {0.f, 0.f, 0.f, 0.f};
#pragma unroll
      for (int w = 0; w < 10; w += 2) {
        o = __builtin_amdgcn_mfma_f32_16x16x32_bf16(pa[w], vb[w], o, 0, 0, 0);
        o2 = __builtin_amdgcn_mfma_f32_16x16x32_bf16(pa[w + 1], vb[w + 1], o2, 0, 0, 0);
      }
      if (nkw == 11) o = __builtin_amdgcn_mfma_f32_16x16x32_bf16(pa[10], vb[10], o, 0, 0, 0);
      o += o2;
    }
    {
      u64* rec = p.ws + PG_OFF + (((size_t)(hkv * 4) * NB + bb) * NSP + sp) * PREC;
#pragma unroll
      for (int h = 0; h < 4; ++h) {
        const u32 mine = f2bf(o[h]);
        const u32 other = dppu<0xB1>(mine);
        if (lane < 16 && (lane & 1) == 0) put_gran(rec + (size_t)h * NB * NSP * PREC + 8 * wave + (lane >> 1), mine | (other << 16));
      }
      if (wave == 0 && lane < 4) {
        float L = 0.f;
#pragma unroll
        for (int w = 0; w < 11; ++w)
          if (w < 10 || nkw == 11) L += ((const float*)(smem + L_SC))[w * 4 + lane];
        put_gran(rec + (size_t)lane * NB * NSP * PREC + 64, __float_as_uint(M));
        put_gran(rec + (size_t)lane * NB * NSP * PREC + 65, __float_as_uint(L));
      }
    }
    if (wave == 0) STAMP(5);
    return;
  }

  const int j = xg + 8 * (y - 24);
  if (wave == 0) STAMP(8);
  if (wave == 0) {
    const u32 tg = __hip_atomic_load((GAS u32*)(p.ws + EP_OFF), RLX_AGENT) + 1u;
    if (lane < 16) ctrl[lane] = lane == 8 ? tg : 0u;
  }
  const bool has_ffn = j < 56;
  const int fcb = j % 28, fks = j / 28;
  const int rg = j >> 3, cg = j & 7;

  const int ocol = 64 * cg + 8 * rg;
  u32 bias = 0;
  if (tid < 896) bias = *(const u32*)(p.b1 + 224 * rg + 2 * (tid % 112));
  if (wave >= 8) {
    const int lw = wave - 8;
#pragma unroll
    for (int i = 0; i < 8; ++i) {
      const int r0 = 64 * i + 8 * lw;
      dma16(p.wq + (size_t)(r0 + (lane >> 3)) * 4096 + 64 * j + 8 * ((lane & 7) ^ wswz(r0 + (lane >> 3))), smem + L_WQ + r0 * 128);
    }
    if (has_ffn) {
#pragma unroll
      for (int i = 0; i < 4; ++i) {
        const int r0 = 64 * i + 8 * lw;
        dma16(p.w1 + (size_t)(256 * fks + r0 + (lane >> 3)) * FF + 64 * fcb + 8 * ((lane & 7) ^ wswz(r0 + (lane >> 3))), smem + L_W1 + r0 * 128);
      }
      asm volatile("s_waitcnt vmcnt(4)" ::: "memory");
    } else {
      vm_wait0();
    }
  } else {
    const u32x4 xa = *(const u32x4*)(p.x + wave * DM + lane * 8);
    float xv[8];
#pragma unroll
    for (int k = 0; k < 4; ++k) { xv[2 * k] = __uint_as_float(xa[k] << 16); xv[2 * k + 1] = __uint_as_float(xa[k] & 0xffff0000u); }
    float s = 0.f;
#pragma unroll
    for (int k = 0; k < 8; ++k) s += xv[k];
    s += dpp<0x121>(s); s += dpp<0x122>(s); s += dpp<0x124>(s); s += dpp<0x128>(s); s = x32_sum(x16_sum(s));
    const float mean = s * (1.f / 512.f);
    float v = 0.f;
#pragma unroll
    for (int k = 0; k < 8; ++k) { const float d = xv[k] - mean; v = fmaf(d, d, v); }
    v += dpp<0x121>(v); v += dpp<0x122>(v); v += dpp<0x124>(v); v += dpp<0x128>(v); v = x32_sum(x16_sum(v));
    const float mb = bfr(mean), vb = bfr(v * (1.f / 512.f));
    const float den = bfr(sqrtf(bfr(vb + 1e-5f)));
    u32x4 o;
#pragma unroll
    for (int k = 0; k < 4; ++k) {
      const float a = bfr(xv[2 * k] - mb) / den, b = bfr(xv[2 * k + 1] - mb) / den;
      o[k] = f2bf(a) | (f2bf(b) << 16);
    }
    *(u32x4*)(smem + L_XN + (wave * XN_STRIDE + lane * 8) * 2) = o;
  }
  lds_wait();
  __builtin_amdgcn_s_barrier();
  const u32 TAG = ctrl[8];
  if (wave == 0) STAMP(9);
  {
    const int nt = wave & 3, kq = wave >> 2;
    bf16x8 af[4], bq[4];
#pragma unroll
    for (int k = 0; k < 4; ++k) {
      bf16x8 a = {0, 0, 0, 0, 0, 0, 0, 0};
      if (m16 < 8) a = *(const bf16x8*)(smem + L_XN + (m16 * XN_STRIDE + (4 * kq + k) * 32 + g4 * 8) * 2);
      af[k] = a;
      bq[k] = bfrag_tr(smem + L_WQ, 4 * kq + k, nt, lane);
    }
    f32x4 d = {0.f, 0.f, 0.f, 0.f};
#pragma unroll
    for (int k = 0; k < 4; ++k) d = __builtin_amdgcn_mfma_f32_16x16x32_bf16(af[k], bq[k], d, 0, 0, 0);
    if (lane < 32) *(f32x4*)(smem + L_PD + ((kq * 4 + nt) * 32 + lane) * 16) = d;
  }
  if (wave == 0) STAMP(19);
  lds_wait();
  __builtin_amdgcn_s_barrier();
  if (wave == 0) STAMP(20);
  if (wave >= 8) { vm_wait0(); lds_arrive(&ctrl[3], lane); }
  if (wave < 4) {
    f32x4 d = {0.f, 0.f, 0.f, 0.f};
    if (lane < 32) {
#pragma unroll
      for (int kq = 0; kq < 4; ++kq) d += *(const f32x4*)(smem + L_PD + ((kq * 4 + wave) * 32 + lane) * 16);
    }
#pragma unroll
    for (int i = 0; i < 4; ++i) {
      const u32 mine = f2bf(d[i]);
      const u32 other = dppu<0xB1>(mine);
      if (lane < 32 && (lane & 1) == 0)
        put_gran(p.ws + QG_OFF + (size_t)(4 * g4 + i) * 2048 + 32 * j + 8 * wave + (m16 >> 1), mine | (other << 16));
    }
  } else if (wave < 8 && has_ffn) {
    lds_spin(&ctrl[3], 8);
    f32x4 e = {0.f, 0.f, 0.f, 0.f};
#pragma unroll
    for (int k = 0; k < 8; ++k) {
      bf16x8 a = {0, 0, 0, 0, 0, 0, 0, 0};
      if (m16 < 8) a = *(const bf16x8*)(smem + L_XN + (m16 * XN_STRIDE + 256 * fks + k * 32 + g4 * 8) * 2);
      e = __builtin_amdgcn_mfma_f32_16x16x32_bf16(a, bfrag_tr(smem + L_W1, k, wave - 4, lane), e, 0, 0, 0);
    }
    if (lane < 32) {
#pragma unroll
      for (int i = 0; i < 4; ++i)
        put_gran(p.ws + HG_OFF + ((size_t)(4 * g4 + i) * 2 + fks) * FF + 64 * fcb + 16 * (wave - 4) + m16, __float_as_uint(e[i]));
    }
  }
  if (wave < 8) lds_arrive(&ctrl[7], lane);
  u32 xr0 = 0, xr1 = 0;
  if (tid < 64) { xr0 = p.x[(tid >> 3) * DM + ocol + (tid & 7)]; xr1 = p.b2[ocol + (tid & 7)]; }
  if (wave == 0) STAMP(10);
  if (wave >= 14) {
    lds_spin(&ctrl[7], 8);
#pragma unroll
    for (int i = 0; i < 46; ++i) {
      const int r0 = 16 * i + 8 * (wave - 14);
      const int r = r0 + (lane >> 3);
      const u16* src = r0 < 512 ? (p.wo + (size_t)(512 * rg + r) * DM) : (p.w2 + (size_t)(224 * rg + r - 512) * DM);
      dma16(src + 64 * cg + 8 * ((lane & 7) ^ wswz(r)), smem + L_W3 + r0 * 128);
    }
    vm_wait0();
    lds_arrive(&ctrl[5], lane);
  }
  if (tid < 896) {
    const int b = tid / 112, jj = tid - 112 * b;
    const u64* g = p.ws + HG_OFF + (size_t)b * 2 * FF + 224 * rg + 2 * jj;
    u64 v[4];
    for (u32 it = 0; it < SPIN_CAP; ++it) {
      v[0] = get_gran(g); v[1] = get_gran(g + 1); v[2] = get_gran(g + FF); v[3] = get_gran(g + FF + 1);
      if (tag_ok(v[0]) && tag_ok(v[1]) && tag_ok(v[2]) && tag_ok(v[3])) break;
      __builtin_amdgcn_s_sleep(4);
    }
    float h0 = __uint_as_float((u32)v[0]) + __uint_as_float((u32)v[2]);
    float h1 = __uint_as_float((u32)v[1]) + __uint_as_float((u32)v[3]);
    h0 = gelu_tanh(bfr(bfr(h0) + bf2f(bias & 0xffffu)));
    h1 = gelu_tanh(bfr(bfr(h1) + bf2f(bias >> 16)));
    ((u32*)(smem + L_A3))[b * (K3_STRIDE / 2) + 256 + jj] = f2bf(h0) | (f2bf(h1) << 16);
  }
  lds_arrive(&ctrl[6], lane);
  lds_spin(&ctrl[6], 16);
  lds_spin(&ctrl[5], 2);
  f32x4 dacc = {0.f, 0.f, 0.f, 0.f};
  bf16x8 b3[16];
  if (wave < 4) {
#pragma unroll
    for (int ks = 16; ks < 23; ++ks) {
      bf16x8 a = {0, 0, 0, 0, 0, 0, 0, 0};
      if (m16 < 8) a = *(const bf16x8*)(smem + L_A3 + (m16 * K3_STRIDE + ks * 32 + g4 * 8) * 2);
      dacc = __builtin_amdgcn_mfma_f32_16x16x32_bf16(a, bfrag_tr(smem + L_W3, ks, wave, lane), dacc, 0, 0, 0);
    }
#pragma unroll
    for (int k = 0; k < 16; ++k) b3[k] = bfrag_tr(smem + L_W3, k, wave, lane);
  }
  if (wave == 0) STAMP(11);
  {
    const int hh = tid >> 8, b = (tid >> 5) & 7, jj = tid & 31;
    const size_t base = PG_OFF + ((size_t)(4 * rg + hh) * NB + b) * NSP * PREC;
    const __amdgpu_buffer_rsrc_t rs = __builtin_amdgcn_make_buffer_rsrc((void*)p.ws, 0, (int)WS_BYTES, 0x00020000);
    u32x4 ov[NSP], ml[NSP];
    for (u32 it = 0; it < SPIN_CAP; ++it) {
      bool ok = true;
#pragma unroll
      for (int s = 0; s < NSP; ++s) {
        ov[s] = get_gran2(rs, base + s * PREC + 2 * jj);
        ml[s] = get_gran2(rs, base + s * PREC + 64);
      }
#pragma unroll
      for (int s = 0; s < NSP; ++s) ok = ok && ov[s][1] == TAG && ov[s][3] == TAG && ml[s][1] == TAG && ml[s][3] == TAG;
      if (__all(ok)) break;
      __builtin_amdgcn_s_sleep(PG_SLEEP);
    }
    float M = -INFINITY;
#pragma unroll
    for (int s = 0; s < NSP; ++s) M = fmaxf(M, __uint_as_float(ml[s][0]));
    float L = 0.f, o[4] = {0.f, 0.f, 0.f, 0.f};
#pragma unroll
    for (int s = 0; s < NSP; ++s) {
      const float wt = __builtin_amdgcn_exp2f((__uint_as_float(ml[s][0]) - M) * LOG2E);
      L = fmaf(wt, __uint_as_float(ml[s][2]), L);
      o[0] = fmaf(wt, bf2f(ov[s][0] & 0xffffu), o[0]);
      o[1] = fmaf(wt, bf2f(ov[s][0] >> 16), o[1]);
      o[2] = fmaf(wt, bf2f(ov[s][2] & 0xffffu), o[2]);
      o[3] = fmaf(wt, bf2f(ov[s][2] >> 16), o[3]);
    }
    const float inv = 1.f / L;
    u32* a3 = (u32*)(smem + L_A3) + b * (K3_STRIDE / 2) + 64 * hh + 2 * jj;
    a3[0] = f2bf(o[0] * inv) | (f2bf(o[1] * inv) << 16);
    a3[1] = f2bf(o[2] * inv) | (f2bf(o[3] * inv) << 16);
  }
  if (wave == 0) STAMP(12);
  lds_wait();
  __builtin_amdgcn_s_barrier();
  if (wave == 0) STAMP(13);
  if (wave < 4) {
    f32x4 d2 = {0.f, 0.f, 0.f, 0.f};
#pragma unroll
    for (int hf = 0; hf < 2; ++hf) {
      bf16x8 af[8];
#pragma unroll
      for (int k = 0; k < 8; ++k) {
        bf16x8 a = {0, 0, 0, 0, 0, 0, 0, 0};
        if (m16 < 8) a = *(const bf16x8*)(smem + L_A3 + (m16 * K3_STRIDE + (8 * hf + k) * 32 + g4 * 8) * 2);
        af[k] = a;
      }
#pragma unroll
      for (int k = 0; k < 8; k += 2) {
        dacc = __builtin_amdgcn_mfma_f32_16x16x32_bf16(af[k], b3[8 * hf + k], dacc, 0, 0, 0);
        d2 = __builtin_amdgcn_mfma_f32_16x16x32_bf16(af[k + 1], b3[8 * hf + k + 1], d2, 0, 0, 0);
      }
      __builtin_amdgcn_sched_barrier(0);
    }
    dacc += d2;
    if (lane < 32) {
      const int col = 16 * wave + m16;
      u64* og = p.ws + OG_OFF + ((((size_t)cg * 8 + (col >> 3)) * 8 + rg) * 8) * 8 + (col & 7);
#pragma unroll
      for (int i = 0; i < 4; ++i) put_gran(og + (4 * g4 + i) * 8, __float_as_uint(dacc[i]));
    }
  }
  if (wave == 0) STAMP(14);
  if (wave == 0) {
    const u64* base = p.ws + OG_OFF + (((size_t)cg * 8 + rg) * 8) * 64 + lane;
    u64 a[8];
    for (u32 it = 0; it < SPIN_CAP; ++it) {
      bool ok = true;
#pragma unroll
      for (int w = 0; w < 8; ++w) a[w] = get_gran(base + w * 64);
#pragma unroll
      for (int w = 0; w < 8; ++w) ok = ok && tag_ok(a[w]);
      if (__all(ok)) break;
      __builtin_amdgcn_s_sleep(1);
    }
    STAMP(15);
    float r = bf2f(xr0) + bf2f(xr1);
#pragma unroll
    for (int w = 0; w < 8; ++w) r += __uint_as_float((u32)a[w]);
    p.out[(lane >> 3) * DM + ocol + (lane & 7)] = (u16)f2bf(r);
    if (j == 0 && lane == 0) __hip_atomic_store((GAS u32*)(p.ws + EP_OFF), TAG, RLX_AGENT);
    STAMP(16);
  }
}

}

extern "C" void kernel_launch(void* const* d_in, const int* in_sizes, int n_in,
                              void* d_out, int out_size, void* d_ws, size_t ws_size,
                              hipStream_t stream) {
  Params p;
  p.x = (const u16*)d_in[0];
  p.kc = (const u16*)d_in[1];
  p.vc = (const u16*)d_in[2];
  p.wq = (const u16*)d_in[3];
  p.wo = (const u16*)d_in[4];
  p.w1 = (const u16*)d_in[6];
  p.b1 = (const u16*)d_in[7];
  p.w2 = (const u16*)d_in[8];
  p.b2 = (const u16*)d_in[9];
  p.out = (u16*)d_out;
  p.ws = (u64*)d_ws;
  hipLaunchKernelGGL(fused_decode, dim3(NWG), dim3(NTHR), 0, stream, p);
}
```

```cpp
#include <hip/hip_runtime.h>
#include <hip/hip_bf16.h>
#include <stdint.h>

typedef unsigned int u32;
typedef unsigned long long u64;
typedef unsigned short u16;
typedef u32 u32x4 __attribute__((ext_vector_type(4)));
typedef float f32x4 __attribute__((ext_vector_type(4)));
typedef short bf16x8 __attribute__((ext_vector_type(8)));
typedef short s16x4 __attribute__((ext_vector_type(4)));

#define GAS __attribute__((address_space(1)))
#define LAS __attribute__((address_space(3)))
#define RLX_AGENT __ATOMIC_RELAXED, __HIP_MEMORY_SCOPE_AGENT

namespace {

constexpr int NB = 8, DM = 512, HQ = 32, HKV = 8, SK = 4096, DH = 128, WINDOW = 1024, FF = 1792;
constexpr int KOFF = SK - WINDOW;
constexpr int NWG = 256, NTHR = 1024;
constexpr u32 SPIN_CAP = 1u << 19;
constexpr float LOG2E = 1.4426950408889634f;

constexpr int NSP = 3;
constexpr int PREC = 66;
constexpr size_t QG_OFF = 0, QG_N = (size_t)NB * 2048;
constexpr size_t HG_OFF = QG_OFF + QG_N, HG_N = (size_t)NB * 2 * FF;
constexpr size_t PG_OFF = HG_OFF + HG_N, PG_N = (size_t)HQ * NB * NSP * PREC;
constexpr size_t OG_OFF = PG_OFF + PG_N, OG_N = (size_t)8 * 8 * 8 * 8 * 8;
constexpr size_t EP_OFF = OG_OFF + OG_N;
constexpr size_t WS_GRAN = EP_OFF + 16;
constexpr size_t WS_BYTES = WS_GRAN * 8;
static_assert(PG_OFF % 2 == 0 && PREC % 2 == 0, "16-byte granule-pair loads");

constexpr int L_CTRL = 0;
constexpr int L_Q = 64;
constexpr int L_ML = L_Q + 1024;
constexpr int L_SC = L_ML + 512;
constexpr int L_PB = L_SC + 2048;
constexpr int L_V = L_PB + 11 * 1024;
constexpr int LA_END = L_V + 352 * 256;
constexpr int XN_STRIDE = 520;
constexpr int K3 = 736, K3_STRIDE = 744;
constexpr int L_XN = 64;
constexpr int L_A3 = L_XN + NB * XN_STRIDE * 2;
constexpr int L_RED = L_A3 + NB * K3_STRIDE * 2;
constexpr int L_WQ = L_RED + 2048;
constexpr int L_W1 = L_WQ + 65536;
constexpr int L_W3 = L_WQ;
constexpr int L_PD = L_W1 + 32768;
constexpr int LB_END = L_PD + 16384;
static_assert(L_W3 + K3 * 128 <= LB_END, "W3 overlay");
constexpr int LDS_BYTES = LA_END > LB_END ? LA_END : LB_END;
static_assert(LDS_BYTES > 80 * 1024 && LDS_BYTES <= 160 * 1024, "one workgroup per CU");
static_assert(L_WQ % 16 == 0 && L_A3 % 16 == 0 && L_V % 16 == 0 && L_PB % 16 == 0 && L_RED % 16 == 0, "align");

struct Params {
  const u16* x; const u16* kc; const u16* vc; const u16* wq; const u16* wo;
  const u16* w1; const u16* b1; const u16* w2; const u16* b2;
  u16* out; u64* ws;
};

__device__ __forceinline__ float bf2f(u32 h) { return __uint_as_float(h << 16); }
__device__ __forceinline__ u32 f2bf(float f) {
  __bf16 h = (__bf16)f;
  return (u32)__builtin_bit_cast(u16, h);
}
__device__ __forceinline__ float bfr(float f) { return bf2f(f2bf(f)); }

template <int CTRL> __device__ __forceinline__ float dpp(float x) {
  return __builtin_bit_cast(float, __builtin_amdgcn_mov_dpp(__builtin_bit_cast(int, x), CTRL, 0xf, 0xf, true));
}
template <int CTRL> __device__ __forceinline__ u32 dppu(u32 x) {
  return (u32)__builtin_amdgcn_mov_dpp((int)x, CTRL, 0xf, 0xf, true);
}
__device__ __forceinline__ float x16_max(float x) {
  auto s = __builtin_amdgcn_permlane16_swap(__float_as_uint(x), __float_as_uint(x), false, false);
  return fmaxf(__uint_as_float(s[0]), __uint_as_float(s[1]));
}
__device__ __forceinline__ float x32_max(float x) {
  auto t = __builtin_amdgcn_permlane32_swap(__float_as_uint(x), __float_as_uint(x), false, false);
  return fmaxf(__uint_as_float(t[0]), __uint_as_float(t[1]));
}
__device__ __forceinline__ float x16_sum(float x) {
  auto s = __builtin_amdgcn_permlane16_swap(__float_as_uint(x), __float_as_uint(x), false, false);
  return __uint_as_float(s[0]) + __uint_as_float(s[1]);
}
__device__ __forceinline__ float x32_sum(float x) {
  auto t = __builtin_amdgcn_permlane32_swap(__float_as_uint(x), __float_as_uint(x), false, false);
  return __uint_as_float(t[0]) + __uint_as_float(t[1]);
}

__device__ __forceinline__ void put_gran_t(u64* g, u32 v, u32 tag) {
  __hip_atomic_store((GAS u64*)g, ((u64)tag << 32) | (u64)v, RLX_AGENT);
}
__device__ __forceinline__ u64 get_gran(const u64* g) {
  return __hip_atomic_load((GAS u64*)g, RLX_AGENT);
}
#define tag_ok(v) ((u32)((v) >> 32) == TAG)
#define put_gran(g, v) put_gran_t((g), (v), TAG)
__device__ __forceinline__ u32x4 get_gran2(__amdgpu_buffer_rsrc_t rs, size_t gidx) {
  return __builtin_amdgcn_raw_buffer_load_b128(rs, (int)(gidx * 8), 0, 16);
}

__device__ __forceinline__ void lds_wait() { asm volatile("s_waitcnt lgkmcnt(0)" ::: "memory"); }
__device__ __forceinline__ void vm_wait0() { asm volatile("s_waitcnt vmcnt(0)" ::: "memory"); }

typedef volatile LAS u32 lctr;
__device__ __forceinline__ void lds_add1(lctr* c) {
  __hip_atomic_fetch_add((LAS u32*)c, 1u, __ATOMIC_RELAXED, __HIP_MEMORY_SCOPE_WORKGROUP);
}
__device__ __forceinline__ void lds_arrive(lctr* c, int lane) {
  lds_wait();
  if (lane == 0) lds_add1(c);
  asm volatile("" ::: "memory");
}
__device__ __forceinline__ void lds_spin(lctr* c, u32 target) {
  for (u32 it = 0; it < SPIN_CAP; ++it) {
    if (*c >= target) break;
    __builtin_amdgcn_s_sleep(1);
  }
  asm volatile("" ::: "memory");
}

__device__ __forceinline__ float gelu_tanh(float v) {
  const float u = 0.7978845608028654f * (v + 0.044715f * v * v * v);
  return 0.5f * v * (1.f + tanhf(u));
}

__device__ __forceinline__ void dma16(const void* gsrc, unsigned char* lds_base) {
  __builtin_amdgcn_global_load_lds(gsrc, (LAS void*)lds_base, 16, 0, 2);
}
__device__ __forceinline__ int wswz(int row) { return (((row >> 1) & 1) | (((row >> 3) & 1) << 1)) << 1; }
__device__ __forceinline__ bf16x8 bfrag_tr(const unsigned char* tile, int ks, int nt, int lane) {
  const int g = lane >> 4, q = (lane & 15) >> 2, pp = lane & 3;
  const int row = 8 * g + q;
  const unsigned char* a = tile + (32 * ks + row) * 128 + (((2 * nt + (pp >> 1)) ^ wswz(row)) * 16) + 8 * (pp & 1);
  const s16x4 lo = __builtin_amdgcn_ds_read_tr16_b64_v4i16((LAS s16x4*)a);
  const s16x4 hi = __builtin_amdgcn_ds_read_tr16_b64_v4i16((LAS s16x4*)(a + 4 * 128));
  return bf16x8{lo[0], lo[1], lo[2], lo[3], hi[0], hi[1], hi[2], hi[3]};
}
__device__ __forceinline__ int vswz(int row) { return ((row & 3) << 2) | ((row >> 2) & 3); }

#define KV_WIN 5
#define Q_SLEEP 2
#define PG_SLEEP 1

__global__ void __launch_bounds__(NTHR, 4) fused_decode(Params p) {
  __shared__ __attribute__((aligned(16))) unsigned char smem[LDS_BYTES];
  lctr* ctrl = (lctr*)(smem + L_CTRL);

  const int tid = threadIdx.x, lane = tid & 63;
  const int wave = __builtin_amdgcn_readfirstlane(tid >> 6);
  const int bid = blockIdx.x, xg = bid & 7, y = bid >> 3;
  const int g4 = lane >> 4, m16 = lane & 15;

  if (y < 24) {
    const int hkv = xg, bb = y / 3, sp = y - 3 * bb;
    const int nkw = sp == 2 ? 10 : 11;
    if (wave > 11 || (wave == 10 && nkw == 10)) return;
    if (wave == 11) {
      const u32 TAG = __hip_atomic_load((GAS u32*)(p.ws + EP_OFF), RLX_AGENT) + 1u;
      if (lane < 16) ctrl[lane] = 0u;
      lds_wait();
      __builtin_amdgcn_s_barrier();
      const u64* g = p.ws + QG_OFF + (size_t)bb * 2048 + hkv * 256 + 4 * lane;
      u64 v[4];
      for (u32 it = 0; it < SPIN_CAP; ++it) {
#pragma unroll
        for (int k = 0; k < 4; ++k) v[k] = get_gran(g + k);
        if (__all(tag_ok(v[0]) && tag_ok(v[1]) && tag_ok(v[2]) && tag_ok(v[3]))) break;
        __builtin_amdgcn_s_sleep(Q_SLEEP);
      }
      *(u32x4*)(smem + L_Q + lane * 16) = u32x4{(u32)v[0], (u32)v[1], (u32)v[2], (u32)v[3]};
      if (lane == 0) ctrl[8] = TAG;
      lds_arrive(&ctrl[0], lane);
      return;
    }
    const size_t kvbase = ((size_t)(bb * HKV + hkv) * SK + KOFF + sp * 352 + wave * 32) * DH;
    const u32x4* kp = (const u32x4*)(p.kc + kvbase + (size_t)m16 * DH + g4 * 8);
    u32x4 kf[8];
#define KV_ISSUE(n) do { \
      if ((n) < 8) { asm volatile("global_load_dwordx4 %0, %1, off nt" : "=v"(kf[(n) & 7]) : "v"(kp + ((n) >> 2) * 16 * (DH / 8) + ((n) & 3) * 4) : "memory"); } \
      else { const int row_ = 32 * wave + 4 * ((n) - 8) + g4; \
             dma16(p.vc + kvbase + (size_t)(4 * ((n) - 8) + g4) * DH + ((m16 ^ vswz(row_)) * 8), smem + L_V + (32 * wave + 4 * ((n) - 8)) * 256); } \
      asm volatile("" ::: "memory"); } while (0)
#pragma unroll
    for (int n = 0; n < KV_WIN; ++n) KV_ISSUE(n);
    __builtin_amdgcn_s_barrier();
#pragma unroll
    for (int n = KV_WIN; n < 16; ++n) {
      asm volatile("s_waitcnt vmcnt(%0)" :: "i"(KV_WIN - 1) : "memory");
      KV_ISSUE(n);
    }
#undef KV_ISSUE
    asm volatile("s_waitcnt vmcnt(8)" : "+v"(kf[0]), "+v"(kf[1]), "+v"(kf[2]), "+v"(kf[3]), "+v"(kf[4]), "+v"(kf[5]), "+v"(kf[6]), "+v"(kf[7]) :: "memory");
    lds_spin(&ctrl[0], 1);
    const u32 TAG = ctrl[8];
    bf16x8 qf[4];
#pragma unroll
    for (int t = 0; t < 4; ++t) {
      bf16x8 z = {0, 0, 0, 0, 0, 0, 0, 0};
      if (m16 < 4) z = *(const bf16x8*)(smem + L_Q + (m16 * 128 + t * 32 + g4 * 8) * 2);
      qf[t] = z;
    }
    constexpr float SCALE = 0.08838834764831845f;
    float a[8];
#pragma unroll
    for (int T = 0; T < 2; ++T) {
      f32x4 s = {0.f, 0.f, 0.f, 0.f};
#pragma unroll
      for (int t = 0; t < 4; ++t) s = __builtin_amdgcn_mfma_f32_16x16x32_bf16(__builtin_bit_cast(bf16x8, kf[4 * T + t]), qf[t], s, 0, 0, 0);
#pragma unroll
      for (int i = 0; i < 4; ++i) a[4 * T + i] = bfr(bfr(s[i]) * SCALE);
    }
    float m = a[0];
#pragma unroll
    for (int i = 1; i < 8; ++i) m = fmaxf(m, a[i]);
    m = x32_max(x16_max(m));
    if (lane < 4) ((float*)(smem + L_ML))[wave * 4 + lane] = m;
    lds_arrive(&ctrl[2], lane);
    lds_spin(&ctrl[2], (u32)nkw);
    float M = m;
#pragma unroll
    for (int w = 0; w < 11; ++w)
      if (w < 10 || nkw == 11) M = fmaxf(M, ((const float*)(smem + L_ML))[w * 4 + (m16 & 3)]);
    const float Ms = M * LOG2E;
    float l = 0.f;
    u32x4 pf;
#pragma unroll
    for (int i = 0; i < 4; ++i) {
      const u32 lo = f2bf(__builtin_amdgcn_exp2f(fmaf(a[2 * i], LOG2E, -Ms))), hi = f2bf(__builtin_amdgcn_exp2f(fmaf(a[2 * i + 1], LOG2E, -Ms)));
      pf[i] = lo | (hi << 16);
      l += bf2f(lo) + bf2f(hi);
    }
    l = x32_sum(x16_sum(l));
    *(u32x4*)(smem + L_PB + (wave * 64 + lane) * 16) = pf;
    if (lane < 4) ((float*)(smem + L_SC))[wave * 4 + lane] = l;
    vm_wait0();
    lds_arrive(&ctrl[1], lane);
    if (wave >= 8) return;
    lds_spin(&ctrl[1], (u32)nkw);
    f32x4 o = {0.f, 0.f, 0.f, 0.f};
    {
      const int q = m16 >> 2, pp = lane & 3;
      const int ch = 2 * wave + (pp >> 1);
      bf16x8 pa[11], vb[11];
#pragma unroll
      for (int w = 0; w < 11; ++w) {
        if (w < 10 || nkw == 11) {
          pa[w] = *(const bf16x8*)(smem + L_PB + (w * 64 + lane) * 16);
          const int r0 = 32 * w + 4 * g4 + q, r1 = r0 + 16;
          const s16x4 lo = __builtin_amdgcn_ds_read_tr16_b64_v4i16((LAS s16x4*)(smem + L_V + r0 * 256 + ((ch ^ vswz(r0)) * 16) + 8 * (pp & 1)));
          const s16x4 hi = __builtin_amdgcn_ds_read_tr16_b64_v4i16((LAS s16x4*)(smem + L_V + r1 * 256 + ((ch ^ vswz(r1)) * 16) + 8 * (pp & 1)));
          vb[w] = bf16x8{lo[0], lo[1], lo[2], lo[3], hi[0], hi[1], hi[2], hi[3]};
        }
      }
      f32x4 o2 = {0.f, 0.f, 0.f, 0.f};
#pragma unroll
      for (int w = 0; w < 10; w += 2) {
        o = __builtin_amdgcn_mfma_f32_16x16x32_bf16(pa[w], vb[w], o, 0, 0, 0);
        o2 = __builtin_amdgcn_mfma_f32_16x16x32_bf16(pa[w + 1], vb[w + 1], o2, 0, 0, 0);
      }
      if (nkw == 11) o = __builtin_amdgcn_mfma_f32_16x16x32_bf16(pa[10], vb[10], o, 0, 0, 0);
      o += o2;
    }
    {
      u64* rec = p.ws + PG_OFF + (((size_t)(hkv * 4) * NB + bb) * NSP + sp) * PREC;
#pragma unroll
      for (int h = 0; h < 4; ++h) {
        const u32 mine = f2bf(o[h]);
        const u32 other = dppu<0xB1>(mine);
        if (lane < 16 && (lane & 1) == 0) put_gran(rec + (size_t)h * NB * NSP * PREC + 8 * wave + (lane >> 1), mine | (other << 16));
      }
      if (wave == 0 && lane < 4) {
        float L = 0.f;
#pragma unroll
        for (int w = 0; w < 11; ++w)
          if (w < 10 || nkw == 11) L += ((const float*)(smem + L_SC))[w * 4 + lane];
        put_gran(rec + (size_t)lane * NB * NSP * PREC + 64, __float_as_uint(M));
        put_gran(rec + (size_t)lane * NB * NSP * PREC + 65, __float_as_uint(L));
      }
    }
    return;
  }

  const int j = xg + 8 * (y - 24);
  if (wave == 0) {
    const u32 tg = __hip_atomic_load((GAS u32*)(p.ws + EP_OFF), RLX_AGENT) + 1u;
    if (lane < 16) ctrl[lane] = lane == 8 ? tg : 0u;
  }
  const bool has_ffn = j < 56;
  const int fcb = j % 28, fks = j / 28;
  const int rg = j >> 3, cg = j & 7;

  const int ocol = 64 * cg + 8 * rg;
  u32 bias = 0;
  if (tid < 896) bias = *(const u32*)(p.b1 + 224 * rg + 2 * (tid % 112));
  if (wave >= 8) {
    const int lw = wave - 8;
#pragma unroll
    for (int i = 0; i < 8; ++i) {
      const int r0 = 64 * i + 8 * lw;
      dma16(p.wq + (size_t)(r0 + (lane >> 3)) * 4096 + 64 * j + 8 * ((lane & 7) ^ wswz(r0 + (lane >> 3))), smem + L_WQ + r0 * 128);
    }
    if (has_ffn) {
#pragma unroll
      for (int i = 0; i < 4; ++i) {
        const int r0 = 64 * i + 8 * lw;
        dma16(p.w1 + (size_t)(256 * fks + r0 + (lane >> 3)) * FF + 64 * fcb + 8 * ((lane & 7) ^ wswz(r0 + (lane >> 3))), smem + L_W1 + r0 * 128);
      }
      asm volatile("s_waitcnt vmcnt(4)" ::: "memory");
    } else {
      vm_wait0();
    }
  } else {
    const u32x4 xa = *(const u32x4*)(p.x + wave * DM + lane * 8);
    float xv[8];
#pragma unroll
    for (int k = 0; k < 4; ++k) { xv[2 * k] = __uint_as_float(xa[k] << 16); xv[2 * k + 1] = __uint_as_float(xa[k] & 0xffff0000u); }
    float s = 0.f;
#pragma unroll
    for (int k = 0; k < 8; ++k) s += xv[k];
    s += dpp<0x121>(s); s += dpp<0x122>(s); s += dpp<0x124>(s); s += dpp<0x128>(s); s = x32_sum(x16_sum(s));
    const float mean = s * (1.f / 512.f);
    float v = 0.f;
#pragma unroll
    for (int k = 0; k < 8; ++k) { const float d = xv[k] - mean; v = fmaf(d, d, v); }
    v += dpp<0x121>(v); v += dpp<0x122>(v); v += dpp<0x124>(v); v += dpp<0x128>(v); v = x32_sum(x16_sum(v));
    const float mb = bfr(mean), vb = bfr(v * (1.f / 512.f));
    const float den = bfr(sqrtf(bfr(vb + 1e-5f)));
    u32x4 o;
#pragma unroll
    for (int k = 0; k < 4; ++k) {
      const float a = bfr(xv[2 * k] - mb) / den, b = bfr(xv[2 * k + 1] - mb) / den;
      o[k] = f2bf(a) | (f2bf(b) << 16);
    }
    *(u32x4*)(smem + L_XN + (wave * XN_STRIDE + lane * 8) * 2) = o;
  }
  lds_wait();
  __builtin_amdgcn_s_barrier();
  const u32 TAG = ctrl[8];
  {
    const int nt = wave & 3, kq = wave >> 2;
    bf16x8 af[4], bq[4];
#pragma unroll
    for (int k = 0; k < 4; ++k) {
      bf16x8 a = {0, 0, 0, 0, 0, 0, 0, 0};
      if (m16 < 8) a = *(const bf16x8*)(smem + L_XN + (m16 * XN_STRIDE + (4 * kq + k) * 32 + g4 * 8) * 2);
      af[k] = a;
      bq[k] = bfrag_tr(smem + L_WQ, 4 * kq + k, nt, lane);
    }
    f32x4 d = {0.f, 0.f, 0.f, 0.f};
#pragma unroll
    for (int k = 0; k < 4; ++k) d = __builtin_amdgcn_mfma_f32_16x16x32_bf16(af[k], bq[k], d, 0, 0, 0);
    if (lane < 32) *(f32x4*)(smem + L_PD + ((kq * 4 + nt) * 32 + lane) * 16) = d;
  }
  lds_wait();
  __builtin_amdgcn_s_barrier();
  if (wave >= 8) { vm_wait0(); lds_arrive(&ctrl[3], lane); }
  if (wave < 4) {
    f32x4 d = {0.f, 0.f, 0.f, 0.f};
    if (lane < 32) {
#pragma unroll
      for (int kq = 0; kq < 4; ++kq) d += *(const f32x4*)(smem + L_PD + ((kq * 4 + wave) * 32 + lane) * 16);
    }
#pragma unroll
    for (int i = 0; i < 4; ++i) {
      const u32 mine = f2bf(d[i]);
      const u32 other = dppu<0xB1>(mine);
      if (lane < 32 && (lane & 1) == 0)
        put_gran(p.ws + QG_OFF + (size_t)(4 * g4 + i) * 2048 + 32 * j + 8 * wave + (m16 >> 1), mine | (other << 16));
    }
  } else if (wave < 8 && has_ffn) {
    lds_spin(&ctrl[3], 8);
    f32x4 e = {0.f, 0.f, 0.f, 0.f};
#pragma unroll
    for (int k = 0; k < 8; ++k) {
      bf16x8 a = {0, 0, 0, 0, 0, 0, 0, 0};
      if (m16 < 8) a = *(const bf16x8*)(smem + L_XN + (m16 * XN_STRIDE + 256 * fks + k * 32 + g4 * 8) * 2);
      e = __builtin_amdgcn_mfma_f32_16x16x32_bf16(a, bfrag_tr(smem + L_W1, k, wave - 4, lane), e, 0, 0, 0);
    }
    if (lane < 32) {
#pragma unroll
      for (int i = 0; i < 4; ++i)
        put_gran(p.ws + HG_OFF + ((size_t)(4 * g4 + i) * 2 + fks) * FF + 64 * fcb + 16 * (wave - 4) + m16, __float_as_uint(e[i]));
    }
  }
  if (wave < 8) lds_arrive(&ctrl[7], lane);
  u32 xr0 = 0, xr1 = 0;
  if (tid < 64) { xr0 = p.x[(tid >> 3) * DM + ocol + (tid & 7)]; xr1 = p.b2[ocol + (tid & 7)]; }
  if (wave >= 14) {
    lds_spin(&ctrl[7], 8);
#pragma unroll
    for (int i = 0; i < 46; ++i) {
      const int r0 = 16 * i + 8 * (wave - 14);
      const int r = r0 + (lane >> 3);
      const u16* src = r0 < 512 ? (p.wo + (size_t)(512 * rg + r) * DM) : (p.w2 + (size_t)(224 * rg + r - 512) * DM);
      dma16(src + 64 * cg + 8 * ((lane & 7) ^ wswz(r)), smem + L_W3 + r0 * 128);
    }
    vm_wait0();
    lds_arrive(&ctrl[5], lane);
  }
  if (tid < 896) {
    const int b = tid / 112, jj = tid - 112 * b;
    const u64* g = p.ws + HG_OFF + (size_t)b * 2 * FF + 224 * rg + 2 * jj;
    u64 v[4];
    for (u32 it = 0; it < SPIN_CAP; ++it) {
      v[0] = get_gran(g); v[1] = get_gran(g + 1); v[2] = get_gran(g + FF); v[3] = get_gran(g + FF + 1);
      if (tag_ok(v[0]) && tag_ok(v[1]) && tag_ok(v[2]) && tag_ok(v[3])) break;
      __builtin_amdgcn_s_sleep(4);
    }
    float h0 = __uint_as_float((u32)v[0]) + __uint_as_float((u32)v[2]);
    float h1 = __uint_as_float((u32)v[1]) + __uint_as_float((u32)v[3]);
    h0 = gelu_tanh(bfr(bfr(h0) + bf2f(bias & 0xffffu)));
    h1 = gelu_tanh(bfr(bfr(h1) + bf2f(bias >> 16)));
    ((u32*)(smem + L_A3))[b * (K3_STRIDE / 2) + 256 + jj] = f2bf(h0) | (f2bf(h1) << 16);
  }
  lds_arrive(&ctrl[6], lane);
  lds_spin(&ctrl[6], 16);
  lds_spin(&ctrl[5], 2);
  f32x4 dacc = {0.f, 0.f, 0.f, 0.f};
  bf16x8 b3[16];
  if (wave < 4) {
#pragma unroll
    for (int ks = 16; ks < 23; ++ks) {
      bf16x8 a = {0, 0, 0, 0, 0, 0, 0, 0};
      if (m16 < 8) a = *(const bf16x8*)(smem + L_A3 + (m16 * K3_STRIDE + ks * 32 + g4 * 8) * 2);
      dacc = __builtin_amdgcn_mfma_f32_16x16x32_bf16(a, bfrag_tr(smem + L_W3, ks, wave, lane), dacc, 0, 0, 0);
    }
#pragma unroll
    for (int k = 0; k < 16; ++k) b3[k] = bfrag_tr(smem + L_W3, k, wave, lane);
  }
  {
    const int hh = tid >> 8, b = (tid >> 5) & 7, jj = tid & 31;
    const size_t base = PG_OFF + ((size_t)(4 * rg + hh) * NB + b) * NSP * PREC;
    const __amdgpu_buffer_rsrc_t rs = __builtin_amdgcn_make_buffer_rsrc((void*)p.ws, 0, (int)WS_BYTES, 0x00020000);
    u32x4 ov[NSP], ml[NSP];
    for (u32 it = 0; it < SPIN_CAP; ++it) {
      bool ok = true;
#pragma unroll
      for (int s = 0; s < NSP; ++s) {
        ov[s] = get_gran2(rs, base + s * PREC + 2 * jj);
        ml[s] = get_gran2(rs, base + s * PREC + 64);
      }
#pragma unroll
      for (int s = 0; s < NSP; ++s) ok = ok && ov[s][1] == TAG && ov[s][3] == TAG && ml[s][1] == TAG && ml[s][3] == TAG;
      if (__all(ok)) break;
      __builtin_amdgcn_s_sleep(PG_SLEEP);
    }
    float M = -INFINITY;
#pragma unroll
    for (int s = 0; s < NSP; ++s) M = fmaxf(M, __uint_as_float(ml[s][0]));
    float L = 0.f, o[4] = {0.f, 0.f, 0.f, 0.f};
#pragma unroll
    for (int s = 0; s < NSP; ++s) {
      const float wt = __builtin_amdgcn_exp2f((__uint_as_float(ml[s][0]) - M) * LOG2E);
      L = fmaf(wt, __uint_as_float(ml[s][2]), L);
      o[0] = fmaf(wt, bf2f(ov[s][0] & 0xffffu), o[0]);
      o[1] = fmaf(wt, bf2f(ov[s][0] >> 16), o[1]);
      o[2] = fmaf(wt, bf2f(ov[s][2] & 0xffffu), o[2]);
      o[3] = fmaf(wt, bf2f(ov[s][2] >> 16), o[3]);
    }
    const float inv = 1.f / L;
    u32* a3 = (u32*)(smem + L_A3) + b * (K3_STRIDE / 2) + 64 * hh + 2 * jj;
    a3[0] = f2bf(o[0] * inv) | (f2bf(o[1] * inv) << 16);
    a3[1] = f2bf(o[2] * inv) | (f2bf(o[3] * inv) << 16);
  }
  lds_wait();
  __builtin_amdgcn_s_barrier();
  if (wave < 4) {
    f32x4 d2 = {0.f, 0.f, 0.f, 0.f};
#pragma unroll
    for (int hf = 0; hf < 2; ++hf) {
      bf16x8 af[8];
#pragma unroll
      for (int k = 0; k < 8; ++k) {
        bf16x8 a = {0, 0, 0, 0, 0, 0, 0, 0};
        if (m16 < 8) a = *(const bf16x8*)(smem + L_A3 + (m16 * K3_STRIDE + (8 * hf + k) * 32 + g4 * 8) * 2);
        af[k] = a;
      }
#pragma unroll
      for (int k = 0; k < 8; k += 2) {
        dacc = __builtin_amdgcn_mfma_f32_16x16x32_bf16(af[k], b3[8 * hf + k], dacc, 0, 0, 0);
        d2 = __builtin_amdgcn_mfma_f32_16x16x32_bf16(af[k + 1], b3[8 * hf + k + 1], d2, 0, 0, 0);
      }
      __builtin_amdgcn_sched_barrier(0);
    }
    dacc += d2;
    if (lane < 32) {
      const int col = 16 * wave + m16;
      u64* og = p.ws + OG_OFF + ((((size_t)cg * 8 + (col >> 3)) * 8 + rg) * 8) * 8 + (col & 7);
#pragma unroll
      for (int i = 0; i < 4; ++i) put_gran(og + (4 * g4 + i) * 8, __float_as_uint(dacc[i]));
    }
  }
  if (wave == 0) {
    const u64* base = p.ws + OG_OFF + (((size_t)cg * 8 + rg) * 8) * 64 + lane;
    u64 a[8];
    for (u32 it = 0; it < SPIN_CAP; ++it) {
      bool ok = true;
#pragma unroll
      for (int w = 0; w < 8; ++w) a[w] = get_gran(base + w * 64);
#pragma unroll
      for (int w = 0; w < 8; ++w) ok = ok && tag_ok(a[w]);
      if (__all(ok)) break;
      __builtin_amdgcn_s_sleep(1);
    }
    float r = bf2f(xr0) + bf2f(xr1);
#pragma unroll
    for (int w = 0; w < 8; ++w) r += __uint_as_float((u32)a[w]);
    p.out[(lane >> 3) * DM + ocol + (lane & 7)] = (u16)f2bf(r);
    if (j == 0 && lane == 0) __hip_atomic_store((GAS u32*)(p.ws + EP_OFF), TAG, RLX_AGENT);
  }
}

}

extern "C" void kernel_launch(void* const* d_in, const int* in_sizes, int n_in,
                              void* d_out, int out_size, void* d_ws, size_t ws_size,
                              hipStream_t stream) {
  Params p;
  p.x = (const u16*)d_in[0];
  p.kc = (const u16*)d_in[1];
  p.vc = (const u16*)d_in[2];
  p.wq = (const u16*)d_in[3];
  p.wo = (const u16*)d_in[4];
  p.w1 = (const u16*)d_in[6];
  p.b1 = (const u16*)d_in[7];
  p.w2 = (const u16*)d_in[8];
  p.b2 = (const u16*)d_in[9];
  p.out = (u16*)d_out;
  p.ws = (u64*)d_ws;
  hipLaunchKernelGGL(fused_decode, dim3(NWG), dim3(NTHR), 0, stream, p);
}
```
